```python
import torch

def _softmax(x, axis=-1):
    return torch.softmax(x, dim=axis)
def _gelu(x):
    return torch.nn.functional.gelu(x, approximate="tanh")
def _silu(x):
    return torch.nn.functional.silu(x)
def _sigmoid(x):
    return torch.sigmoid(x)
def _layernorm(x, axis=-1, eps=1e-5):
    m = x.mean(dim=axis, keepdim=True)
    v = x.var(dim=axis, keepdim=True, unbiased=False)
    return (x - m) / torch.sqrt(v + eps)
def _scatter_add(tgt, idx, src, axis=0):
    return tgt.clone().index_add_(axis, idx, src)
def _segment_sum(src, seg, n):
    out = torch.zeros((n,) + src.shape[1:], dtype=src.dtype)
    return out.index_add_(0, seg, src)
def _pad(x, pw):
    flat = tuple(v for pair in reversed(pw) for v in pair)
    return torch.nn.functional.pad(x, flat)

kernel_name = 'standalone__gqa_block_cached__near_ridge'

def setup_inputs():

    _w = (lambda a: (lambda t: t.to(torch.bfloat16) if t.is_floating_point() else t)(torch.as_tensor(a)))
    B, Sq, D = 8, 48, 512
    Hq, Hkv, Sk, Dh = 32, 8, 2048, 128
    assert Hq % Hkv == 0, (Hq, Hkv)
    return {
        "x": _w((lambda *s: torch.randn(*s, dtype=torch.bfloat16) * 0.8)(B, Sq, D)),
        "k_cache": _w((lambda *s: torch.randn(*s, dtype=torch.bfloat16) * 0.8)(B, Hkv, Sk, Dh)),
        "v_cache": _w((lambda *s: torch.randn(*s, dtype=torch.bfloat16) * 0.05)(B, Hkv, Sk, Dh)),
        "wq": _w((lambda *s: torch.randn(*s, dtype=torch.bfloat16) * 0.8)(D, Hq * Dh) * (D ** -0.5)),
        "wo": _w((lambda *s: torch.randn(*s, dtype=torch.bfloat16) * 0.05)(Hq * Dh, D) * ((Hq * Dh) ** -0.5)),
    }

def reference(x, k_cache, v_cache, wq, wo):
    B, Sq, D = x.shape
    Hq, Dh = 32, 128
    Hkv = k_cache.shape[1]
    rep = Hq // Hkv
    q = (lambda x, *d: x.permute(*d))((x @ wq).reshape(B, Sq, Hq, Dh), 0, 2, 1, 3)
    k = (lambda x, n, axis: torch.repeat_interleave(x, n, dim=axis))(k_cache, rep, axis=1)
    v = (lambda x, n, axis: torch.repeat_interleave(x, n, dim=axis))(v_cache, rep, axis=1)
    scores = torch.einsum("bhqd,bhkd->bhqk", q, k) * (Dh ** -0.5)
    p = _softmax(scores, axis=-1)
    o = torch.einsum("bhqk,bhkd->bhqd", p, v)
    return (lambda x, *d: x.permute(*d))(o, 0, 2, 1, 3).reshape(B, Sq, Hq * Dh) @ wo
```

```cpp
#include <hip/hip_runtime.h>
#include <hip/hip_bf16.h>
typedef unsigned short u16;
constexpr int B=8, SQ=48, DM=512, HQ=32, HKV=8, SK=2048, DH=128;
__device__ __forceinline__ float bf2f(u16 v){ return __uint_as_float(((unsigned)v)<<16); }
__device__ __forceinline__ u16 f2bf(float f){ __hip_bfloat16 h=__float2bfloat16(f); return *reinterpret_cast<u16*>(&h); }

__global__ void qproj(const u16* x, const u16* wq, u16* q){
  int n = blockIdx.x*256+threadIdx.x;
  int m = blockIdx.y;
  float acc=0;
  for(int k=0;k<DM;k++) acc += bf2f(x[m*DM+k])*bf2f(wq[(size_t)k*4096+n]);
  q[(size_t)m*4096+n]=f2bf(acc);
}
__global__ void attn(const u16* q, const u16* kc, const u16* vc, u16* o){
  __shared__ float sq[128];
  __shared__ float sp[2048];
  __shared__ float red[128];
  int row = blockIdx.x;
  int h = blockIdx.y; int b = blockIdx.z;
  int t = threadIdx.x;
  int kvh = h/4;
  sq[t] = bf2f(q[((size_t)(b*SQ+row))*4096 + h*128 + t]);
  __syncthreads();
  const u16* K = kc + ((size_t)(b*HKV+kvh))*SK*DH;
  const u16* V = vc + ((size_t)(b*HKV+kvh))*SK*DH;
  float mx=-1e30f;
  for(int k=t;k<SK;k+=128){
    float a=0;
    for(int d=0;d<128;d++) a+= sq[d]*bf2f(K[(size_t)k*128+d]);
    a*=0.08838834764831845f;
    sp[k]=a; mx=fmaxf(mx,a);
  }
  red[t]=mx; __syncthreads();
  for(int s=64;s>0;s>>=1){ if(t<s) red[t]=fmaxf(red[t],red[t+s]); __syncthreads(); }
  mx=red[0]; __syncthreads();
  float sum=0;
  for(int k=t;k<SK;k+=128){ float e=__expf(sp[k]-mx); sp[k]=e; sum+=e; }
  red[t]=sum; __syncthreads();
  for(int s=64;s>0;s>>=1){ if(t<s) red[t]+=red[t+s]; __syncthreads(); }
  sum=red[0];
  float acc=0;
  for(int k=0;k<SK;k++) acc+= sp[k]*bf2f(V[(size_t)k*128+t]);
  o[((size_t)(b*SQ+row))*4096 + h*128 + t]=f2bf(acc/sum);
}
__global__ void oproj(const u16* o, const u16* wo, u16* out){
  int n = blockIdx.x*256+threadIdx.x;
  int m = blockIdx.y;
  float acc=0;
  for(int k=0;k<4096;k++) acc += bf2f(o[(size_t)m*4096+k])*bf2f(wo[(size_t)k*512+n]);
  out[(size_t)m*512+n]=f2bf(acc);
}
extern "C" void kernel_launch(void* const* d_in, const int* in_sizes, int n_in, void* d_out, int out_size, void* d_ws, size_t ws_size, hipStream_t stream){
  const u16* x=(const u16*)d_in[0]; const u16* kc=(const u16*)d_in[1]; const u16* vc=(const u16*)d_in[2];
  const u16* wq=(const u16*)d_in[3]; const u16* wo=(const u16*)d_in[4];
  u16* q=(u16*)d_ws; u16* o=q+384*4096;
  qproj<<<dim3(16,384),256,0,stream>>>(x,wq,q);
  attn<<<dim3(48,32,8),128,0,stream>>>(q,kc,vc,o);
  oproj<<<dim3(2,384),256,0,stream>>>(o,wo,(u16*)d_out);
}
```

```cpp
#include <hip/hip_runtime.h>
#include <stdint.h>

typedef unsigned int u32;
typedef unsigned long long u64;
typedef unsigned short u16;
typedef float f32x4 __attribute__((ext_vector_type(4)));
typedef float f32x2 __attribute__((ext_vector_type(2)));
typedef u32 u32x4 __attribute__((ext_vector_type(4)));
typedef u32 u32x2 __attribute__((ext_vector_type(2)));
typedef short s16x4 __attribute__((ext_vector_type(4)));
typedef __bf16 bf16x8 __attribute__((ext_vector_type(8)));
typedef __bf16 bf16x2 __attribute__((ext_vector_type(2)));
#define LAS __attribute__((address_space(3)))
#define GAS __attribute__((address_space(1)))
#define CAS __attribute__((address_space(4)))
#define DEVI __device__ __forceinline__

constexpr u32 WS_G = 0;
constexpr u32 WS_QF = 4096;
constexpr u32 WS_OF = 16384;
constexpr u32 WS_QX = 65536;
constexpr u32 WS_ML = 4u << 20;
constexpr u32 WS_OP = 8u << 20;
constexpr u32 WS_END = 24u << 20;

constexpr int SL = 16384;
constexpr int BUN = 22528;
constexpr int KS_[8] = {7, 8, 9, 3, 5, 1, 7, 8};
constexpr int VS_[8] = {6, 4, 0, 2, 6, 9, 4, 3};
constexpr int WS_[32] = {0, 5, 2, 1, 4, 3, 6, 0, 5, 1, 2, 3, 4, 0, 6, 7, 8, 9, 1, 5, 2, 3, 0, 4, 6, 7, 8, 9, 1, 5, 2, 3};
constexpr int BF_OFF = 7 * SL;
constexpr int WT_OFF = 6 * SL;
constexpr int YV_[16] = {16, 12, 12, 12, 14, 26, 24, 24, 26, 26, 26, 24, 26, 18, 10, 0};

DEVI f32x4 mfma(u32x4 a, u32x4 b, f32x4 c) {
  return __builtin_amdgcn_mfma_f32_16x16x32_bf16(__builtin_bit_cast(bf16x8, a), __builtin_bit_cast(bf16x8, b), c, 0, 0, 0);
}
DEVI u32 pk(float a, float b) {
  f32x2 v = {a, b};
  return __builtin_bit_cast(u32, __builtin_convertvector(v, bf16x2));
}
DEVI float bflo(u32 v) { return __uint_as_float(v << 16); }
DEVI float bfhi(u32 v) { return __uint_as_float(v & 0xffff0000u); }
DEVI u32x4 lds128(u32 a) { return *(const LAS u32x4*)(uintptr_t)a; }
DEVI void st128(u32 a, u32x4 v) { *(LAS u32x4*)(uintptr_t)a = v; }
DEVI void st64(u32 a, u32x2 v) { *(LAS u32x2*)(uintptr_t)a = v; }
DEVI u32x2 ldstr(u32 a) {
  typedef short v4i16 __attribute__((ext_vector_type(4)));
  return __builtin_bit_cast(u32x2, __builtin_amdgcn_ds_read_tr16_b64_v4i16((LAS v4i16*)(uintptr_t)a));
}
DEVI u32x4 ldstr2(u32 a0, u32 a1) {
  u32x2 x = ldstr(a0), y = ldstr(a1);
  u32x4 r = {x[0], x[1], y[0], y[1]};
  return r;
}
DEVI float ex2(float x) { return __builtin_amdgcn_exp2f(x); }
DEVI float xmax(float v) {
  auto r = __builtin_amdgcn_permlane16_swap(__float_as_uint(v), __float_as_uint(v), false, false);
  v = fmaxf(__uint_as_float(r[0]), __uint_as_float(r[1]));
  auto q = __builtin_amdgcn_permlane32_swap(__float_as_uint(v), __float_as_uint(v), false, false);
  return fmaxf(__uint_as_float(q[0]), __uint_as_float(q[1]));
}
DEVI float xsum(float v) {
  auto r = __builtin_amdgcn_permlane16_swap(__float_as_uint(v), __float_as_uint(v), false, false);
  v = __uint_as_float(r[0]) + __uint_as_float(r[1]);
  auto q = __builtin_amdgcn_permlane32_swap(__float_as_uint(v), __float_as_uint(v), false, false);
  return __uint_as_float(q[0]) + __uint_as_float(q[1]);
}

#define DMA1(POL, M, VO, SB)                                                                     \
  asm volatile("s_mov_b32 m0, %0\n\ts_nop 0\n\tglobal_load_lds_dwordx4 %1, %2" POL ::"s"(M), "v"(VO), "s"(SB) : "memory")
#define DMA2(POL, M, VO, SB)                                                                     \
  asm volatile("s_mov_b32 m0, %0\n\ts_nop 0\n\tglobal_load_lds_dwordx4 %1, %2" POL               \
               "\n\tglobal_load_lds_dwordx4 %1, %2 offset:2048" POL ::"s"(M), "v"(VO), "s"(SB) : "memory")
#define DMA4K(POL, M, V0, V1, V2, V3, SB)                                                        \
  asm volatile("s_mov_b32 m0, %0\n\ts_nop 0\n\tglobal_load_lds_dwordx4 %1, %5" POL               \
               "\n\tglobal_load_lds_dwordx4 %2, %5 offset:1024" POL                              \
               "\n\tglobal_load_lds_dwordx4 %3, %5 offset:2048" POL                              \
               "\n\tglobal_load_lds_dwordx4 %4, %5 offset:3072" POL ::"s"(M), "v"(V0), "v"(V1), "v"(V2), "v"(V3), "s"(SB) : "memory")
#define KVPOL " nt"
#define WAITV(N) asm volatile("s_waitcnt vmcnt(%0)" ::"i"(N) : "memory")
#define PIN(x) asm volatile("" : "+v"(x))
#define RELANE()                                                                                                   \
  do {                                                                                                             \
    asm volatile("v_mbcnt_lo_u32_b32 %0, -1, 0\n\tv_mbcnt_hi_u32_b32 %0, -1, %0" : "=v"(lane));                    \
    l16 = lane & 15u; g4 = lane >> 4;                                                                              \
  } while (0)
#define BAR() asm volatile("s_waitcnt lgkmcnt(0)\n\ts_barrier" ::: "memory")

#define STAMP(i)

constexpr u32 SPIN_MAX = 1u << 16;

__global__ __launch_bounds__(512, 2) void gqa_fused(const u16* __restrict__ X, const u16* __restrict__ Kc, const u16* __restrict__ Vc,
                                                    const u16* __restrict__ Wq, const u16* __restrict__ Wo, u16* __restrict__ Out,
                                                    unsigned char* __restrict__ ws) {
  extern __shared__ __attribute__((aligned(16))) unsigned char smem[];
  const int wave = __builtin_amdgcn_readfirstlane(threadIdx.x >> 6);
  u32 lane, l16, g4;
  RELANE();
  const int w4 = wave & 3, hf = wave >> 2;
  const int bid = blockIdx.x;
  const int kvh = bid & 7, s = (bid >> 3) & 3, b = bid >> 5;
  const int grp = b * 8 + kvh;
  const u32 L0 = (u32)(uintptr_t)(LAS unsigned char*)smem;
  const u64 tag = *(const CAS u64*)(uintptr_t)(ws + WS_G + b * 128) + 1ull;
  const u32 tag32 = (u32)tag;
  const __amdgpu_buffer_rsrc_t rs = __builtin_amdgcn_make_buffer_rsrc(ws, 0, WS_END, 0x00020000);
  STAMP(0);

  const u32 kvo = 4096u * w4 + 256u * g4 + 16u * (u32)(l16 ^ g4);
  const u32 vlp = 256u * ((lane >> 1) & 7) + 32u * g4 + 16u * (lane & 1);
  const unsigned char* Kg = (const unsigned char*)Kc + (size_t)(grp * 2048 + s * 512) * 256;
  const unsigned char* Vg = (const unsigned char*)Vc + (size_t)(grp * 2048 + s * 512) * 256;
  const unsigned char* Wog = (const unsigned char*)Wo + (size_t)(kvh * 512) * 1024;

  auto issueK = [&](int r, int slot) {
    u32 k0 = kvo;
    PIN(k0);
    DMA4K(KVPOL, L0 + slot * SL + 4096u * w4, k0, k0 ^ 64u, k0 ^ 128u, k0 ^ 192u, Kg + r * 16384);
  };
  auto issueV = [&](int r, int slot) {
    const u32 vo = vlp + 4096u * w4;
    DMA2(KVPOL, L0 + slot * SL + 4096u * w4, vo, Vg + r * 16384);
    DMA2(KVPOL, L0 + slot * SL + 4096u * w4 + 1024u, vo, Vg + r * 16384 + 128);
  };
  u32 wlp = 0;
  auto issueW = [&](int t) {
    const unsigned char* sb = Wog + (size_t)(t & 7) * 65536 + (t >> 3) * 256;
    const u32 m = L0 + WS_[t] * SL + 4096u * w4;
    DMA1("", m, wlp, sb);
    DMA1("", m + 1024u, wlp, sb + 128);
    DMA1("", m + 2048u, wlp, sb + 8192);
    DMA1("", m + 3072u, wlp, sb + 8192 + 128);
  };

  const int h = kvh * 4 + s;
  const u32 qlp0 = 8192u * ((lane >> 1) & 7) + 32u * g4 + 16u * (lane & 1);
  const u32 qlp1 = 8192u * (((lane >> 1) & 7) ^ 4) + 32u * g4 + 16u * (lane & 1);
  const u32 xlp = 1024u * (lane >> 3) + 16u * (u32)((lane & 7) ^ g4 ^ (4 * (w4 & 1)));
  auto issueB = [&](int t) {
    const int kt = (t + b) & 7;
    const u32 reg = L0 + (t & 3) * BUN;
    const unsigned char* sb = (const unsigned char*)Wq + (size_t)(64 * kt + 16 * w4) * 8192 + h * 256;
    DMA1("", reg + 4096u * w4, qlp0, sb);
    DMA1("", reg + 4096u * w4 + 1024u, qlp0, sb + 128);
    DMA1("", reg + 4096u * w4 + 2048u, qlp1, sb + 8 * 8192);
    DMA1("", reg + 4096u * w4 + 3072u, qlp1, sb + 8 * 8192 + 128);
    const unsigned char* xb = (const unsigned char*)X + (size_t)(48 * b) * 1024 + kt * 128;
    DMA1("", reg + 16384u + 1024u * w4, xlp, xb + w4 * 8192);
    DMA1("", reg + 16384u + 1024u * (4 + (w4 & 1)), xlp, xb + (4 + (w4 & 1)) * 8192);
  };
  if (hf == 0) {
    issueB(0); issueB(1); issueB(2); issueB(3);
  } else {
    issueK(0, KS_[0]); issueK(1, KS_[1]); issueK(2, KS_[2]); issueV(0, VS_[0]);
  }
  {
    f32x4 qa[3] = {{0, 0, 0, 0}, {0, 0, 0, 0}, {0, 0, 0, 0}};
    const u32 ab0 = L0 + 2048u * g4 + 256u * wave + 128u * (g4 & 1) + 32u * ((lane >> 2) & 3) + 8u * (lane & 3);
    const u32 ab1 = ab0 ^ 128u;
    const u32 xb0 = L0 + 16384u + 128u * l16 + 16u * (u32)(g4 ^ (l16 >> 1));
    const u32 xb1 = xb0 ^ 64u;
#pragma unroll
    for (int t = 0; t < 8; t++) {
      if (hf == 0) {
        constexpr int yc[8] = {3, 2, 2, 2, 2, 2, 1, 0};
        switch (yc[t]) { case 3: WAITV(18); break; case 2: WAITV(12); break; case 1: WAITV(6); break; default: WAITV(0); }
      }
      BAR();
      if (t == 0) STAMP(1);
      if (hf == 0 && t >= 1 && t + 3 < 8) issueB(t + 3);
      const u32 reg = (t & 3) * BUN;
#pragma unroll
      for (int i = 0; i < 2; i++) {
        u32x4 a = ldstr2(ab0 + reg + 8192 * i, ab1 + reg + 8192 * i);
#pragma unroll
        for (int rt = 0; rt < 3; rt++) {
          u32x4 xf = lds128((i ? xb1 : xb0) + reg + 2048 * rt);
          qa[rt] = mfma(a, xf, qa[rt]);
        }
      }
    }
    const float c = 0.08838834764831845f * 1.4426950408889634f;
    const u32 qw = L0 + (u32)(((wave >> 1) * 64 + (2 * (wave & 1) + (g4 >> 1)) * 16 + l16) * 16 + 8 * (g4 & 1));
#pragma unroll
    for (int rt = 0; rt < 3; rt++) {
      u32x2 v = {pk(qa[rt][0] * c, qa[rt][1] * c), pk(qa[rt][2] * c, qa[rt][3] * c)};
      st64(qw + rt * 4096, v);
    }
  }
  BAR();
  STAMP(2);
  u32 warm0 = 0, warm1 = 0;
  if (hf == 0) {
#pragma unroll
    for (int f = 0; f < 3; f++) {
      u32x4 v = lds128(L0 + (3 * w4 + f) * 1024 + lane * 16);
      __builtin_amdgcn_raw_buffer_store_b128(v, rs, WS_QX + (grp * 4 + s) * 12288 + (3 * w4 + f) * 1024 + lane * 16, 0, 16);
    }
    WAITV(0);
    GAS u64* qf = (GAS u64*)(ws + WS_QF) + grp * 16;
    if (lane == 0) __hip_atomic_store(qf + s * 4 + w4, tag, __ATOMIC_RELAXED, __HIP_MEMORY_SCOPE_AGENT);
    STAMP(3);
    u32 sp = 0;
    for (;;) {
      bool ok = true;
      if (lane < 4 && lane != (u32)s) ok = __hip_atomic_load(qf + lane * 4 + w4, __ATOMIC_RELAXED, __HIP_MEMORY_SCOPE_AGENT) == tag;
      if (__all(ok)) break;
      if (++sp > SPIN_MAX) break;
      __builtin_amdgcn_s_sleep(1);
    }
    STAMP(4);
    const u32 lo = lane * 16u;
#pragma unroll
    for (int o = 1; o < 4; o++) {
      const unsigned char* sb = ws + WS_QX + (size_t)(grp * 4 + ((s + o) & 3)) * 12288 + w4 * 3072;
      const u32 m = L0 + o * 12288 + w4 * 3072;
      DMA1(" sc1", m, lo, sb);
      DMA1(" sc1", m + 1024u, lo, sb + 1024);
      DMA1(" sc1", m + 2048u, lo, sb + 2048);
    }
    WAITV(0);
  } else {
    {
      const int wt = b * 4 + s;
      const unsigned char* sb = Wog + (size_t)(wt & 7) * 65536 + (wt >> 3) * 256;
      const u32 wo_ = (u32)(w4 * 16 + (lane >> 2)) * 1024u + (lane & 1) * 128u;
      asm volatile("global_load_dword %0, %1, %2" : "=v"(warm0) : "v"(wo_), "s"(sb) : "memory");
      asm volatile("global_load_dword %0, %1, %2 offset:64" : "=v"(warm1) : "v"(wo_), "s"(sb) : "memory");
    }
    issueK(3, KS_[3]); issueV(1, VS_[1]); issueK(4, KS_[4]);
    WAITV(22);
  }
  BAR();
  STAMP(5);

  RELANE();
  const int g = w4;
  u32x4 qf_[3][4];
  {
    const u32 qb = L0 + (u32)((g - s) & 3) * 12288u + lane * 16u;
#pragma unroll
    for (int rt = 0; rt < 3; rt++)
#pragma unroll
      for (int ks = 0; ks < 4; ks++) qf_[rt][ks] = lds128(qb + (rt * 4 + ks) * 1024);
  }
  u32 kcur = L0 + 8192u * hf + 256u * l16 + 16u * (u32)(g4 ^ l16);
  u32 vb = L0 + 8192u * hf + 2048u * (g4 >> 1) + 128u * (g4 & 1) + 32u * ((lane >> 2) & 3) + 8u * (lane & 3);
  f32x4 O[8][3];
#pragma unroll
  for (int db = 0; db < 8; db++)
#pragma unroll
    for (int rt = 0; rt < 3; rt++) O[db][rt] = f32x4{0, 0, 0, 0};
  f32x4 negm[3];
  float lsum[3] = {0, 0, 0};
  u32x4 kpre[4];

  u32 kad[4];
  auto qk = [&](int r, f32x4 (&S)[2][3], bool first) {
#pragma unroll
    for (int kb = 0; kb < 2; kb++)
#pragma unroll
      for (int ks = 0; ks < 4; ks++) {
        u32x4 kf = (kb == 0) ? kpre[ks] : lds128(kad[ks] + 4096);
#pragma unroll
        for (int rt = 0; rt < 3; rt++) {
          f32x4 cin = (ks == 0) ? (first ? f32x4{0, 0, 0, 0} : negm[rt]) : S[kb][rt];
          S[kb][rt] = mfma(kf, qf_[rt][ks], cin);
        }
      }
  };
  auto soft = [&](f32x4 (&S)[2][3], u32x4 (&P)[3]) {
#pragma unroll
    for (int rt = 0; rt < 3; rt++) {
      float e[8];
#pragma unroll
      for (int kb = 0; kb < 2; kb++)
#pragma unroll
        for (int j = 0; j < 4; j++) { e[kb * 4 + j] = ex2(S[kb][rt][j]); }
      lsum[rt] += ((e[0] + e[1]) + (e[2] + e[3])) + ((e[4] + e[5]) + (e[6] + e[7]));
      PIN(lsum[rt]);
      P[rt] = u32x4{pk(e[0], e[1]), pk(e[2], e[3]), pk(e[4], e[5]), pk(e[6], e[7])};
    }
  };
  auto pv = [&](int r, u32x4 (&P)[3]) {
    vb += (u32)((VS_[r] - (r ? VS_[r ? r - 1 : 0] : 0)) * SL);
    PIN(vb);
#pragma unroll
    for (int db = 0; db < 8; db++) {
      const u32 a = vb + 256 * db;
      u32x4 vf = ldstr2(a, a + 4096);
#pragma unroll
      for (int rt = 0; rt < 3; rt++) O[db][rt] = mfma(vf, P[rt], O[db][rt]);
    }
  };
  auto prefK = [&](int r) {
    kcur += (u32)((KS_[r] - (r ? KS_[r ? r - 1 : 0] : 0)) * SL);
    PIN(kcur);
#pragma unroll
    for (int ks = 0; ks < 4; ks++) {
      kad[ks] = kcur ^ (64u * ks);
      kpre[ks] = lds128(kad[ks]);
    }
  };

  u32x4 Pa[3], Pb[3], Pc[3];
  {
    prefK(0);
    f32x4 S[2][3];
    qk(0, S, true);
#pragma unroll
    for (int rt = 0; rt < 3; rt++) {
      float m = fmaxf(fmaxf(fmaxf(S[0][rt][0], S[0][rt][1]), fmaxf(S[0][rt][2], S[0][rt][3])),
                      fmaxf(fmaxf(S[1][rt][0], S[1][rt][1]), fmaxf(S[1][rt][2], S[1][rt][3])));
      m = xmax(m);
      negm[rt] = f32x4{-m, -m, -m, -m};
      S[0][rt] -= m; S[1][rt] -= m;
    }
    prefK(1);
    soft(S, Pa);
  }
#define PHASE(R, PIN, POUT, VMC, ISSUE)            \
  {                                                \
    if (hf) WAITV(VMC);                            \
    BAR();                                         \
    STAMP(5 + R);                                  \
    if (hf) { ISSUE; }                             \
    f32x4 S[2][3];                                 \
    qk(R, S, false);                               \
    soft(S, POUT);                                 \
    pv(R - 1, PIN);                                \
    prefK(R + 1);                                  \
  }
  PHASE(1, Pa, Pb, 14, (issueV(2, VS_[2]), issueK(5, KS_[5]), issueV(3, VS_[3]), issueK(6, KS_[6])));
  asm volatile("" ::"v"(warm0), "v"(warm1));
  PHASE(2, Pb, Pa, 20, (issueV(4, VS_[4]), issueK(7, KS_[7])));
  PHASE(3, Pa, Pb, 20, (issueV(5, VS_[5]), issueV(6, VS_[6])));
  RELANE();
  wlp = 1024u * ((lane >> 1) & 7) + 32u * g4 + 16u * (lane & 1) + 16384u * w4;
  PHASE(4, Pb, Pa, 20, (issueV(7, VS_[7]), issueW(0)));
  PHASE(5, Pa, Pb, 20, (issueW(1), issueW(2)));
  bool bad;
  float mrow[3];
  {
    if (hf) WAITV(20);
    BAR();
    STAMP(11);
    if (hf) issueW(3);
    {
      f32x4 S[2][3];
      qk(6, S, false);
      soft(S, Pa);
      pv(5, Pb);
      prefK(7);
    }
    f32x4 S[2][3];
    qk(7, S, false);
    soft(S, Pc);
    bool bl = false;
#pragma unroll
    for (int rt = 0; rt < 3; rt++) {
      lsum[rt] = xsum(lsum[rt]);
      float nm = negm[rt][0];
      PIN(nm);
      mrow[rt] = -nm;
      bl |= !(lsum[rt] < 1073741824.0f);
    }
    bad = __any(bl);
  }
  RELANE();
  const u32 mlw = WS_ML + (u32)(((grp * 4 + g) * 8 + 2 * s + hf) * 48 + 16 * g4 + l16) * 16u;
  auto publish = [&]() {
    float m0 = mrow[0], m1 = mrow[1], m2 = mrow[2];
    float l0 = lsum[0], l1 = lsum[1], l2 = lsum[2];
    PIN(m0); PIN(m1); PIN(m2); PIN(l0); PIN(l1); PIN(l2);
    float m = m2, l = l2;
    if (g4 == 1) { m = m1; l = l1; }
    if (g4 == 0) { m = m0; l = l0; }
    u32x4 v = {__float_as_uint(m), tag32, __float_as_uint(l), tag32};
    if (g4 < 3) __builtin_amdgcn_raw_buffer_store_b128(v, rs, mlw, 0, 16);
  };
  if (!bad) publish();
  if (hf) WAITV(16);
  BAR();
  STAMP(12);
  pv(6, Pa);
  pv(7, Pc);
  if (bad) {
    u32x4 q2[3][4];
#pragma unroll
    for (int rt = 0; rt < 3; rt++)
#pragma unroll
      for (int ks = 0; ks < 4; ks++)
        q2[rt][ks] = __builtin_amdgcn_raw_buffer_load_b128(rs, WS_QX + (grp * 4 + g) * 12288 + (rt * 4 + ks) * 1024 + lane * 16, 0, 16);
#pragma unroll
    for (int db = 0; db < 8; db++)
#pragma unroll
      for (int rt = 0; rt < 3; rt++) O[db][rt] = f32x4{0, 0, 0, 0};
#pragma unroll
    for (int rt = 0; rt < 3; rt++) { mrow[rt] = -3.0e38f; lsum[rt] = 0.f; }
#pragma unroll 1
    for (int r = 0; r < 8; r++) {
      const int key0 = r * 64 + hf * 32;
      f32x4 S[2][3];
#pragma unroll
      for (int kb = 0; kb < 2; kb++) {
#pragma unroll
        for (int rt = 0; rt < 3; rt++) S[kb][rt] = f32x4{0, 0, 0, 0};
#pragma unroll
        for (int ks = 0; ks < 4; ks++) {
          u32x4 kf = *(const GAS u32x4*)(Kg + (size_t)(key0 + 16 * kb + l16) * 256 + 64 * ks + 16 * g4);
#pragma unroll
          for (int rt = 0; rt < 3; rt++) S[kb][rt] = mfma(kf, q2[rt][ks], S[kb][rt]);
        }
      }
      u32x4 P[3];
#pragma unroll
      for (int rt = 0; rt < 3; rt++) {
        float m = fmaxf(fmaxf(fmaxf(S[0][rt][0], S[0][rt][1]), fmaxf(S[0][rt][2], S[0][rt][3])),
                        fmaxf(fmaxf(S[1][rt][0], S[1][rt][1]), fmaxf(S[1][rt][2], S[1][rt][3])));
        m = fmaxf(xmax(m), mrow[rt]);
        const float al = ex2(mrow[rt] - m);
        mrow[rt] = m;
        float e[8];
#pragma unroll
        for (int kb = 0; kb < 2; kb++)
#pragma unroll
          for (int j = 0; j < 4; j++) e[kb * 4 + j] = ex2(S[kb][rt][j] - m);
        lsum[rt] = lsum[rt] * al + (((e[0] + e[1]) + (e[2] + e[3])) + ((e[4] + e[5]) + (e[6] + e[7])));
        P[rt] = u32x4{pk(e[0], e[1]), pk(e[2], e[3]), pk(e[4], e[5]), pk(e[6], e[7])};
#pragma unroll
        for (int db = 0; db < 8; db++) O[db][rt] *= al;
      }
#pragma unroll 1
      for (int dh = 0; dh < 1; dh++) {
#pragma unroll
        for (int db = 0; db < 8; db++) {
          const GAS u16* vp = (const GAS u16*)(Vg + (size_t)(key0 + 4 * g4) * 256) + 16 * db + l16;
          u32x4 vf;
#pragma unroll
          for (int jj = 0; jj < 4; jj++) {
            const int k0 = (jj >> 1) * 16 + (jj & 1) * 2;
            vf[jj] = (u32)vp[k0 * 128] | ((u32)vp[(k0 + 1) * 128] << 16);
          }
#pragma unroll
          for (int rt = 0; rt < 3; rt++) O[db][rt] = mfma(vf, P[rt], O[db][rt]);
        }
      }
    }
#pragma unroll
    for (int rt = 0; rt < 3; rt++) lsum[rt] = xsum(lsum[rt]);
    publish();
  }
  STAMP(13);
  RELANE();
  u32 bfw = L0 + BF_OFF + (u32)g * 12288u + lane * 16u;
  PIN(bfw);
#define SENDH(HF)                                                                                \
  _Pragma("unroll") for (int ii = 0; ii < 2; ii++)                                               \
  _Pragma("unroll") for (int rt = 0; rt < 3; rt++) {                                             \
    const f32x4 a = O[(HF ? 0 : 4) + 2 * ii][rt], c = O[(HF ? 0 : 4) + 2 * ii + 1][rt];          \
    u32x4 v = {pk(a[0], a[1]), pk(a[2], a[3]), pk(c[0], c[1]), pk(c[2], c[3])};                  \
    st128(bfw + (u32)(((HF ? 0 : 2) + ii) * 3 + rt) * 1024u, v);                                 \
  }
  if (hf) { SENDH(1) } else { SENDH(0) }
  if (hf == 0) {
    u32x4 gm[3][2];
    u32 sp = 0;
    const u32 mlr = WS_ML + (u32)(((grp * 4 + g) * 8 + g4) * 48 + l16) * 16u;
    for (;;) {
      bool ok = true;
#pragma unroll
      for (int rt = 0; rt < 3; rt++)
#pragma unroll
        for (int sp2 = 0; sp2 < 2; sp2++) {
          gm[rt][sp2] = __builtin_amdgcn_raw_buffer_load_b128(rs, mlr + (sp2 * 4 * 48 + rt * 16) * 16, 0, 16);
        }
#pragma unroll
      for (int rt = 0; rt < 3; rt++)
#pragma unroll
        for (int sp2 = 0; sp2 < 2; sp2++) ok &= (gm[rt][sp2][1] == tag32) & (gm[rt][sp2][3] == tag32);
      if (__all(ok)) break;
      if (++sp > SPIN_MAX) break;
      __builtin_amdgcn_s_sleep(1);
    }
#pragma unroll
    for (int rt = 0; rt < 3; rt++) {
      const float m0 = __uint_as_float(gm[rt][0][0]), m1 = __uint_as_float(gm[rt][1][0]);
      const float M = xmax(fmaxf(m0, m1));
      const float e0 = ex2(m0 - M), e1 = ex2(m1 - M);
      const float L = xsum(e0 * __uint_as_float(gm[rt][0][2]) + e1 * __uint_as_float(gm[rt][1][2]));
      const float wgt = ((s >> 1) ? e1 : e0) / L;
      if ((g4 >> 1) == (u32)(s & 1)) *(LAS float*)(uintptr_t)(L0 + WT_OFF + (u32)((g * 2 + (g4 & 1)) * 48 + rt * 16 + l16) * 4u) = wgt;
    }
  }
  BAR();
  STAMP(14);
  if (hf) { issueW(4); issueW(5); }
#define MERGEH(HF)                                                                                                              \
  _Pragma("unroll") for (int rt = 0; rt < 3; rt++) {                                                                            \
    const float wo_ = *(const LAS float*)(uintptr_t)(L0 + WT_OFF + (u32)((g * 2 + HF) * 48 + rt * 16 + l16) * 4u);              \
    const float wp_ = *(const LAS float*)(uintptr_t)(L0 + WT_OFF + (u32)((g * 2 + (HF ^ 1)) * 48 + rt * 16 + l16) * 4u);        \
    _Pragma("unroll") for (int ii = 0; ii < 2; ii++) {                                                                          \
      const u32 ad = bfw + (u32)((2 * HF + ii) * 3 + rt) * 1024u;                                                               \
      const u32x4 p = lds128(ad);                                                                                               \
      const f32x4 a = O[4 * HF + 2 * ii][rt], c = O[4 * HF + 2 * ii + 1][rt];                                                   \
      u32x4 v = {pk(a[0] * wo_ + bflo(p[0]) * wp_, a[1] * wo_ + bfhi(p[0]) * wp_),                                              \
                 pk(a[2] * wo_ + bflo(p[1]) * wp_, a[3] * wo_ + bfhi(p[1]) * wp_),                                              \
                 pk(c[0] * wo_ + bflo(p[2]) * wp_, c[1] * wo_ + bfhi(p[2]) * wp_),                                              \
                 pk(c[2] * wo_ + bflo(p[3]) * wp_, c[3] * wo_ + bfhi(p[3]) * wp_)};                                             \
      st128(ad, v);                                                                                                             \
    }                                                                                                                           \
  }
  if (hf) { MERGEH(1) } else { MERGEH(0) }
  RELANE();
  u32x4 Bf[4][4][3];
  const u32 wb0 = L0 + 256u * wave + 2048u * (g4 >> 1) + 128u * (g4 & 1) + 32u * ((lane >> 2) & 3) + 8u * (lane & 3);
  u32 bfr = L0 + BF_OFF + lane * 16u;
  PIN(bfr);
  GAS u64* of = (GAS u64*)(ws + WS_OF) + b * 128;
  const int src = kvh * 4 + s;
  f32x4 acc[3];
#pragma unroll
  for (int j = 0; j < 16; j++) {
    if (hf) {
      switch (YV_[j]) {
        case 0: WAITV(0); break; case 10: WAITV(10); break; case 12: WAITV(12); break; case 14: WAITV(14); break; case 16: WAITV(16); break;
        case 18: WAITV(18); break; case 24: WAITV(24); break; default: WAITV(26);
      }
    } else if (j >= 7 && (j & 3) == 3) WAITV(0);
    BAR();
    if (j == 0) STAMP(15);
    if (j >= 7 && (j & 3) == 3 && (wave == 0 && lane == 0)) __hip_atomic_store(of + ((j - 7) >> 2) * 32 + src, tag, __ATOMIC_RELAXED, __HIP_MEMORY_SCOPE_AGENT);
    if (hf) {
      if (j == 0) issueW(6);
      else if (j == 4) { issueW(13); issueW(14); issueW(15); issueW(16); issueW(17); }
      else if (j < 4) { issueW(5 + 2 * j); issueW(6 + 2 * j); }
      else if (j <= 11) { issueW(8 + 2 * j); issueW(9 + 2 * j); }
    }
    const int hg = j & 3;
    u32 wad = 0;
    if (hg == 0) {
#pragma unroll
      for (int rt = 0; rt < 3; rt++) acc[rt] = f32x4{0, 0, 0, 0};
    }
#pragma unroll
    for (int I = 0; I < 4; I++) {
      if (j < 4) {
#pragma unroll
        for (int rt = 0; rt < 3; rt++) Bf[hg][I][rt] = lds128(bfr + (u32)((hg * 4 + I) * 3 + rt) * 1024u);
      }
      if ((I & 1) == 0) { wad = wb0 + WS_[2 * j + (I >> 1)] * SL; PIN(wad); }
      const u32 a = wad + 8192 * (I & 1);
      u32x4 wf = ldstr2(a, a + 4096);
#pragma unroll
      for (int rt = 0; rt < 3; rt++) acc[rt] = mfma(wf, Bf[hg][I][rt], acc[rt]);
    }
    if (hg == 3) {
      const int q = j >> 2;
      u32 p0[2] = {pk(acc[0][0], acc[0][1]), pk(acc[0][2], acc[0][3])};
      u32 p1[2] = {pk(acc[1][0], acc[1][1]), pk(acc[1][2], acc[1][3])};
      u32 p2[2] = {pk(acc[2][0], acc[2][1]), pk(acc[2][2], acc[2][3])};
      auto s0 = __builtin_amdgcn_permlane16_swap(p0[0], p1[0], false, false);
      auto s1 = __builtin_amdgcn_permlane16_swap(p0[1], p1[1], false, false);
      auto t0 = __builtin_amdgcn_permlane16_swap(p2[0], p2[0], false, false);
      auto t1 = __builtin_amdgcn_permlane16_swap(p2[1], p2[1], false, false);
      u32x4 va = {s0[0], s1[0], s0[1], s1[1]};
      u32x4 vb = {t0[0], t1[0], t0[1], t1[1]};
      const u32 ob = WS_OP + (u32)((b * 4 + q) * 32 + src) * 12288u + (u32)wave * 1536u;
      __builtin_amdgcn_raw_buffer_store_b128(va, rs, ob + lane * 16, 0, 16);
      if ((g4 & 1) == 0) __builtin_amdgcn_raw_buffer_store_b128(vb, rs, ob + 1024 + ((g4 >> 1) * 16 + l16) * 16, 0, 16);
    }
  }
  STAMP(16);
  RELANE();
  WAITV(0);
  BAR();
  if ((wave == 0 && lane == 0)) __hip_atomic_store(of + 3 * 32 + src, tag, __ATOMIC_RELAXED, __HIP_MEMORY_SCOPE_AGENT);
  STAMP(17);
  if (hf == 0) {
    const int q = w4;
    u32 sp = 0;
    for (;;) {
      bool ok = true;
      if (lane < 32) ok = __hip_atomic_load(of + q * 32 + lane, __ATOMIC_RELAXED, __HIP_MEMORY_SCOPE_AGENT) == tag;
      if (__all(ok)) break;
      if (++sp > SPIN_MAX) break;
      __builtin_amdgcn_s_sleep(1);
    }
    STAMP(18);
    if (q == 3 && src == 0 && lane == 0) __hip_atomic_store((GAS u64*)(ws + WS_G + b * 128), tag, __ATOMIC_RELAXED, __HIP_MEMORY_SCOPE_AGENT);
    const int c = lane & 31, sg = lane >> 5;
    const int cc = c < 24 ? c : 23;
    const int gi = src * 24 + cc;
    const u32 rb = WS_OP + (u32)((b * 4 + q) * 32 + sg * 16) * 12288u + (u32)gi * 16u;
    u32x4 d[16];
#pragma unroll
    for (int i = 0; i < 16; i++) d[i] = __builtin_amdgcn_raw_buffer_load_b128(rs, rb + i * 12288, 0, 16);
    float a[8] = {0, 0, 0, 0, 0, 0, 0, 0};
#pragma unroll
    for (int i = 0; i < 16; i++)
#pragma unroll
      for (int e = 0; e < 4; e++) { a[2 * e] += bflo(d[i][e]); a[2 * e + 1] += bfhi(d[i][e]); }
#pragma unroll
    for (int e = 0; e < 8; e++) {
      auto r = __builtin_amdgcn_permlane32_swap(__float_as_uint(a[e]), __float_as_uint(a[e]), false, false);
      a[e] = __uint_as_float(r[0]) + __uint_as_float(r[1]);
    }
    const int wv = gi / 96, rem = gi - wv * 96;
    int row, cg;
    if (rem < 64) { row = 16 * ((rem >> 4) & 1) + (rem & 15); cg = 2 * wv + (rem >> 5); }
    else { const int r2 = rem - 64; row = 32 + (r2 & 15); cg = 2 * wv + (r2 >> 4); }
    u32x4 o = {pk(a[0], a[1]), pk(a[2], a[3]), pk(a[4], a[5]), pk(a[6], a[7])};
    if (lane < 24) *(GAS u32x4*)(Out + (size_t)(b * 48 + row) * 512 + q * 128 + cg * 8) = o;
  }
  STAMP(19);
}

extern "C" void kernel_launch(void* const* d_in, const int* in_sizes, int n_in, void* d_out, int out_size, void* d_ws, size_t ws_size,
                              hipStream_t stream) {
  (void)in_sizes; (void)n_in; (void)out_size; (void)ws_size;
  (void)hipFuncSetAttribute((const void*)gqa_fused, hipFuncAttributeMaxDynamicSharedMemorySize, 163840);
  gqa_fused<<<256, 512, 163840, stream>>>((const u16*)d_in[0], (const u16*)d_in[1], (const u16*)d_in[2], (const u16*)d_in[3],
                                          (const u16*)d_in[4], (u16*)d_out, (unsigned char*)d_ws);
}
```

```cpp
#include <hip/hip_runtime.h>
#include <stdint.h>

typedef unsigned int u32;
typedef unsigned long long u64;
typedef unsigned short u16;
typedef float f32x4 __attribute__((ext_vector_type(4)));
typedef float f32x2 __attribute__((ext_vector_type(2)));
typedef u32 u32x8 __attribute__((ext_vector_type(8)));
typedef u32 u32x4 __attribute__((ext_vector_type(4)));
typedef u32 u32x2 __attribute__((ext_vector_type(2)));
typedef __bf16 bf16x8 __attribute__((ext_vector_type(8)));
typedef __bf16 bf16x2 __attribute__((ext_vector_type(2)));
#define LAS __attribute__((address_space(3)))
#define GAS __attribute__((address_space(1)))
#define CAS __attribute__((address_space(4)))
#define DEVI __device__ __forceinline__

constexpr u32 WS_G = 0;
constexpr u32 WS_HB = 32768;
constexpr u32 WS_HA = 65536;
constexpr u32 WS_OL = 512u << 10;
constexpr u32 WS_OX = 1u << 20;
constexpr u32 WS_OP = 9u << 20;
constexpr u32 WS_END = 14u << 20;

constexpr u32 SA = 0, SBK = 32768, SC0 = 65536, SC1 = 98304, SC2 = 131072;
constexpr u32 XOFF = 32768;
constexpr u32 RSP = 81920;
constexpr u32 MLO = 49152;
constexpr u32 SCR = 57344;
constexpr u32 REDO = 65536;
constexpr u32 KS_(int n) { return (n & 1) ? SBK : SA; }
constexpr u32 VS_(int n) { return (n + 1) % 3 == 0 ? SC0 : (n + 1) % 3 == 1 ? SC1 : SC2; }
constexpr u32 TS_(int t) { return t % 3 == 0 ? SA : t % 3 == 1 ? SC1 : SC2; }

DEVI f32x4 mfma(u32x4 a, u32x4 b, f32x4 c) {
  return __builtin_amdgcn_mfma_f32_16x16x32_bf16(__builtin_bit_cast(bf16x8, a), __builtin_bit_cast(bf16x8, b), c, 0, 0, 0);
}
DEVI u32 pk(float a, float b) {
  f32x2 v = {a, b};
  return __builtin_bit_cast(u32, __builtin_convertvector(v, bf16x2));
}
DEVI float bflo(u32 v) { return __uint_as_float(v << 16); }
DEVI float bfhi(u32 v) { return __uint_as_float(v & 0xffff0000u); }
DEVI u32x4 lds128(u32 a) { return *(const LAS u32x4*)(uintptr_t)a; }
DEVI f32x4 lds128f(u32 a) { return *(const LAS f32x4*)(uintptr_t)a; }
DEVI void st128(u32 a, u32x4 v) { *(LAS u32x4*)(uintptr_t)a = v; }
DEVI void st128f(u32 a, f32x4 v) { *(LAS f32x4*)(uintptr_t)a = v; }
DEVI void st64(u32 a, u32x2 v) { *(LAS u32x2*)(uintptr_t)a = v; }
DEVI u32x2 ldstr(u32 a) {
  typedef short v4i16 __attribute__((ext_vector_type(4)));
  return __builtin_bit_cast(u32x2, __builtin_amdgcn_ds_read_tr16_b64_v4i16((LAS v4i16*)(uintptr_t)a));
}
DEVI u32x4 ldstr2(u32 a0, u32 a1) {
  u32x2 x = ldstr(a0), y = ldstr(a1);
  u32x4 r = {x[0], x[1], y[0], y[1]};
  return r;
}
DEVI float ex2(float x) { return __builtin_amdgcn_exp2f(x); }
DEVI float lg2(float x) { return __builtin_amdgcn_logf(x); }
DEVI float xmax(float v) {
  auto r = __builtin_amdgcn_permlane16_swap(__float_as_uint(v), __float_as_uint(v), false, false);
  v = fmaxf(__uint_as_float(r[0]), __uint_as_float(r[1]));
  auto q = __builtin_amdgcn_permlane32_swap(__float_as_uint(v), __float_as_uint(v), false, false);
  return fmaxf(__uint_as_float(q[0]), __uint_as_float(q[1]));
}
DEVI float xsum(float v) {
  auto r = __builtin_amdgcn_permlane16_swap(__float_as_uint(v), __float_as_uint(v), false, false);
  v = __uint_as_float(r[0]) + __uint_as_float(r[1]);
  auto q = __builtin_amdgcn_permlane32_swap(__float_as_uint(v), __float_as_uint(v), false, false);
  return __uint_as_float(q[0]) + __uint_as_float(q[1]);
}

#define KVPOL " nt"
#define DMA1(POL, M, VO, SB_, OFF)                                                               \
  asm volatile("s_mov_b32 m0, %0\n\ts_nop 0\n\tglobal_load_lds_dwordx4 %1, %2 offset:%3" POL ::"s"(M), "v"(VO), "s"(SB_), "i"(OFF) : "memory")
#define GLD(POL, D, VO, SB_, OFF)                                                                \
  asm volatile("global_load_dwordx4 %0, %1, %2 offset:%3" POL : "=v"(D) : "v"(VO), "s"(SB_), "i"(OFF) : "memory")
#define WAITV(N) asm volatile("s_waitcnt vmcnt(%0)" ::"i"(N) : "memory")
#define WAITL() asm volatile("s_waitcnt lgkmcnt(0)" ::: "memory")
#define PIN(x) asm volatile("" : "+v"(x))
#define SB() __builtin_amdgcn_sched_barrier(0)
#define RELANE()                                                                                                   \
  do {                                                                                                             \
    asm volatile("v_mbcnt_lo_u32_b32 %0, -1, 0\n\tv_mbcnt_hi_u32_b32 %0, -1, %0" : "=v"(lane));                    \
    l16 = lane & 15u; g4 = lane >> 4;                                                                              \
  } while (0)
#define BAR() asm volatile("s_waitcnt lgkmcnt(0)\n\ts_barrier" ::: "memory")
#define BARV(N) asm volatile("s_waitcnt vmcnt(%0) lgkmcnt(0)\n\ts_barrier" ::"i"(N) : "memory")
#define BARVN(N) asm volatile("s_waitcnt vmcnt(%0)\n\ts_barrier" ::"i"(N) : "memory")

#define STAMP(i)
#define STVAL(i, x)
#define STCLK(i)

template <int V> struct IC_ { static constexpr int value = V; };
template <int I, int N, class F> DEVI void sfor(F&& f) {
  if constexpr (I < N) { f(IC_<I>{}); sfor<I + 1, N>(f); }
}
#define EXP0 8
#define EXPN 11
#define MUPOS 39
constexpr int expAt(int i) { for (int e = 0; e < 24; e++) if (EXP0 + (e * EXPN) / 8 == i) return e; return -1; }
constexpr int cvtAt(int i) { for (int e = 1; e < 24; e += 2) if (EXP0 + 2 + (e * EXPN) / 8 == i) return e; return -1; }
constexpr int DPOS(int d) { return d < 4 ? 1 + 2 * d : 3 + 2 * d; }

constexpr u32 SPIN_MAX = 1u << 14;
constexpr u32 HINT_MAX = 4000;

__global__ __launch_bounds__(512, 2) void gqa_fused(const u16* __restrict__ X, const u16* __restrict__ Kc, const u16* __restrict__ Vc,
                                                    const u16* __restrict__ Wq, const u16* __restrict__ Wo, u16* __restrict__ Out,
                                                    unsigned char* __restrict__ ws) {
  extern __shared__ __attribute__((aligned(16))) unsigned char smem[];
  const int wave = __builtin_amdgcn_readfirstlane(threadIdx.x >> 6);
  u32 lane, l16, g4;
  RELANE();
  const int q4 = wave & 3, hd = wave >> 2;
  const int bid = blockIdx.x;
  const int kvh = bid & 7, j = (bid >> 3) & 3, b = bid >> 5;
  const int hp = j & 1, kh = j >> 1;
  const int grp = b * 8 + kvh;
  const u32 L0 = (u32)(uintptr_t)(LAS unsigned char*)smem;
  const u64 tag = *(const CAS u64*)(uintptr_t)(ws + WS_G + b * 128) + 1ull;
  const u32 tag32 = (u32)tag;
  const __amdgpu_buffer_rsrc_t rs = __builtin_amdgcn_make_buffer_rsrc(ws, 0, WS_END, 0x00020000);
  STAMP(0); STCLK(23);

  const u32 kvo = 4096u * wave + 256u * (lane & 7) + 16u * (lane >> 3);
  const u32 vo0 = 4096u * wave + 256u * ((lane >> 1) & 7) + 32u * (g4 ^ (u32)(2 * (wave >> 1))) + 16u * (lane & 1);
  const u32 vo1 = vo0 ^ 128u;
  const unsigned char* Kg = (const unsigned char*)Kc + (size_t)(grp * 2048 + kh * 1024) * 256;
  const unsigned char* Vg = (const unsigned char*)Vc + (size_t)(grp * 2048 + kh * 1024) * 256;
  const u32 Lw = L0 + 4096u * wave;
#define IK(r, p) DMA1(KVPOL, Lw + KS_(r), kvo, Kg + (r) * 32768 - (((p) & 1) ? 896 : 0), 1024 * (p))
#define IV(r, p) DMA1(KVPOL, Lw + VS_(r), (((p) & 1) ? vo1 : vo0), Vg + (r) * 32768 - (((p) & 1) ? 1024 : 0), 1024 * (p))

  u32x4 kf[8];
  u32x4 qf[3][4];
  {
    const int cq = q4, k2 = hd;
    const u32 wq0 = 8192u * ((lane >> 1) & 7) + 32u * g4 + 16u * (lane & 1), wq1 = wq0 ^ 32768u;
    const unsigned char* wqb = (const unsigned char*)Wq + (size_t)(k2 * 256) * 8192 + ((4 * kvh + 2 * hp) * 128 + 64 * cq) * 2;
    auto issueWq = [&](int t) {
      const unsigned char* sb = wqb + (size_t)((t + b) & 7) * (32 * 8192);
      DMA1("", Lw + TS_(t), wq0, sb, 0);
      DMA1("", Lw + TS_(t), wq1, sb + 65536 - 1024, 1024);
      DMA1("", Lw + TS_(t), wq0, sb + 2 * 65536 - 2048, 2048);
      DMA1("", Lw + TS_(t), wq1, sb + 3 * 65536 - 3072, 3072);
    };
    {
      const u32 xo = 1024u * (lane & 7) + 16u * (lane >> 3);
      const unsigned char* xb = (const unsigned char*)X + (size_t)(48 * b) * 1024 + wave * 128;
#pragma unroll
      for (int p = 0; p < 6; p++) DMA1("", L0 + XOFF + 6144u * wave + 1024u * p, xo, xb + p * 8192, 0);
    }
    issueWq(0); issueWq(1); issueWq(2);
    {
      const u32 k0o = 256u * (32u * q4 + l16) + 16u * g4;
#pragma unroll
      for (int ks = 0; ks < 4; ks++) GLD("", kf[ks], k0o, Kg, 64 * ks);
#pragma unroll
      for (int ks = 0; ks < 4; ks++) GLD("", kf[4 + ks], k0o, Kg + 4096, 64 * ks);
    }
    u32x4 sgk[4], sgv[4];
    f32x4 qa[4][3];
    const u32 abase = Lw + 1024u * g4 + 128u * (g4 & 1) + 32u * ((lane >> 2) & 3) + 8u * (lane & 3);
    const u32 akeep = abase + 512u * k2, asend = abase + 512u * (k2 ^ 1);
    const u32 xbase = L0 + XOFF + 1024u * (l16 >> 3) + 128u * g4 + 16u * (l16 & 7) + 4u * 6144u * k2;
    auto qstep = [&](int t) {
      const int kk = (t + b) & 7;
      const u32 xa = xbase + (u32)((kk >> 1) * 6144 + (kk & 1) * 512);
      u32x4 a[4], xf[3];
#pragma unroll
      for (int c = 0; c < 4; c++) {
        const u32 a0 = ((c & 2) ? asend : akeep) + TS_(t) + 256u * (c & 1);
        a[c] = ldstr2(a0, a0 ^ 128u);
      }
#pragma unroll
      for (int rt = 0; rt < 3; rt++) xf[rt] = lds128(xa + 2048 * rt);
#pragma unroll
      for (int c = 0; c < 4; c++)
#pragma unroll
        for (int rt = 0; rt < 3; rt++) qa[c][rt] = mfma(a[c], xf[rt], t ? qa[c][rt] : f32x4{0, 0, 0, 0});
      WAITL();
    };
    BARVN(16);
    STAMP(1);
    qstep(0); issueWq(3);
    GLD(KVPOL, sgk[0], kvo, Kg + 32768, 0); GLD(KVPOL, sgk[1], kvo, Kg + 32768, 128);
    GLD(KVPOL, sgk[2], kvo, Kg + 32768, 2048); GLD(KVPOL, sgk[3], kvo, Kg + 32768, 2176);
    WAITV(20);
    qstep(1); issueWq(4);
    GLD(KVPOL, sgv[0], vo0, Vg, 0); GLD(KVPOL, sgv[1], vo1, Vg, 0);
    GLD(KVPOL, sgv[2], vo0, Vg, 2048); GLD(KVPOL, sgv[3], vo1, Vg, 2048);
    WAITV(24);
    qstep(2); issueWq(5);
    WAITV(16);
    qstep(3); issueWq(6);
    WAITV(12);
    qstep(4); issueWq(7);
    WAITV(8);
    qstep(5);
    IV(1, 0); IV(1, 1); IV(1, 2); IV(1, 3);
    WAITV(8);
    qstep(6);
    asm volatile("s_waitcnt vmcnt(4)"
                 : "+v"(sgk[0]), "+v"(sgk[1]), "+v"(sgk[2]), "+v"(sgk[3]), "+v"(sgv[0]), "+v"(sgv[1]), "+v"(sgv[2]), "+v"(sgv[3]),
                   "+v"(kf[0]), "+v"(kf[1]), "+v"(kf[2]), "+v"(kf[3]), "+v"(kf[4]), "+v"(kf[5]), "+v"(kf[6]), "+v"(kf[7])::"memory");
    qstep(7);
    STAMP(2);
    {
      const u32 lw = Lw + lane * 16u;
      const u32 sw = L0 + RSP + 2048u * wave + lane * 16u;
#pragma unroll
      for (int i = 0; i < 6; i++) st128f(i < 4 ? lw + SA + 1024 * i : sw + 1024 * (i - 4), qa[2 + i / 3][i % 3]);
#pragma unroll
      for (int p = 0; p < 4; p++) st128(lw + VS_(0) + 1024 * p, sgv[p]);
    }
    BAR();
    {
      const u32 pw = L0 + 4096u * (wave ^ 4);
      const u32 lr = pw + lane * 16u;
      const u32 sr = L0 + RSP + 2048u * (wave ^ 4) + lane * 16u;
      f32x4 o[6];
#pragma unroll
      for (int i = 0; i < 6; i++) o[i] = lds128f(i < 4 ? lr + SA + 1024 * i : sr + 1024 * (i - 4));
      const u32 lw = Lw + lane * 16u;
#pragma unroll
      for (int p = 0; p < 4; p++) st128(lw + KS_(1) + 1024 * p, sgk[p]);
      const float c = 0.08838834764831845f * 1.4426950408889634f;
      const u32 qw = pw + SA + 256u * (g4 >> 1) + 16u * l16 + 8u * (g4 & 1);
#pragma unroll
      for (int i = 0; i < 6; i++) {
        const f32x4 q = (qa[i / 3][i % 3] + o[i]) * c;
        st64(qw + 1024 * (i % 3) + 512 * (i / 3), u32x2{pk(q[0], q[1]), pk(q[2], q[3])});
      }
    }
    BAR();
    STAMP(3);
    const u32 qr = L0 + SA + 8192u * hd + lane * 16u;
#pragma unroll
    for (int ks = 0; ks < 4; ks++)
#pragma unroll
      for (int rt = 0; rt < 3; rt++) qf[rt][ks] = lds128(qr + 4096 * (ks >> 1) + 16384 * ((ks & 1) ^ 1) + 1024 * rt);
    BAR();
    IK(2, 0); IK(2, 1); IK(2, 2); IK(2, 3);
    IV(2, 0); IV(2, 1); IV(2, 2); IV(2, 3);
  }
  STAMP(5);
  u32 kaddr = L0 + 8192u * q4 + 2048u * (l16 >> 3) + 128u * g4 + 16u * (l16 & 7);
  u32 vb = L0 + 8192u * q4 + 2048u * (g4 >> 1) + 128u * (g4 & 1) + 32u * ((lane >> 2) & 3) + 8u * (lane & 3) + VS_(0);
  f32x4 O[8][3];
  f32x4 negm;
  float ls[3] = {0.f, 0.f, 0.f};
  u32x4 P[3];
  u32x4 ones = {0x3f803f80u, 0x3f803f80u, 0x3f803f80u, 0x3f803f80u};
  PIN(ones);
  {
    f32x4 S0[3], S1[3];
#pragma unroll
    for (int rt = 0; rt < 3; rt++)
#pragma unroll
      for (int ks = 0; ks < 4; ks++) S0[rt] = mfma(kf[ks], qf[rt][ks], ks ? S0[rt] : f32x4{0, 0, 0, 0});
#pragma unroll
    for (int rt = 0; rt < 3; rt++)
#pragma unroll
      for (int ks = 0; ks < 4; ks++) S1[rt] = mfma(kf[4 + ks], qf[rt][ks], ks ? S1[rt] : f32x4{0, 0, 0, 0});
    SB();
#pragma unroll
    for (int i = 0; i < 8; i++) kf[i] = lds128(kaddr + KS_(1) + 4096 * (i >> 2) + 512 * (i & 3));
    float m = -3.0e38f;
#pragma unroll
    for (int rt = 0; rt < 3; rt++)
      m = fmaxf(m, fmaxf(fmaxf(fmaxf(S0[rt][0], S0[rt][1]), fmaxf(S0[rt][2], S0[rt][3])),
                         fmaxf(fmaxf(S1[rt][0], S1[rt][1]), fmaxf(S1[rt][2], S1[rt][3]))));
    m = xmax(m);
    negm = f32x4{-m, -m, -m, -m};
#pragma unroll
    for (int rt = 0; rt < 3; rt++) {
      P[rt] = u32x4{pk(ex2(S0[rt][0] - m), ex2(S0[rt][1] - m)), pk(ex2(S0[rt][2] - m), ex2(S0[rt][3] - m)),
                    pk(ex2(S1[rt][0] - m), ex2(S1[rt][1] - m)), pk(ex2(S1[rt][2] - m), ex2(S1[rt][3] - m))};
    }
  }
  STCLK(21);
  const unsigned char* Wog = (const unsigned char*)Wo + (size_t)(kvh * 512 + 8 * (wave >> 1)) * 1024 + 256 * j + 128 * (wave & 1);
  u32 wlp = 0;
#define IW(u) DMA1("", L0 + 1024u * wave + 8192u * (u), wlp, Wog + (u) * 32768, 0)
#define PHASE(R, BV, MV, D0, D1, D2, D3, D4, D5, D6, D7)                                                  \
  {                                                                                                       \
    asm volatile("s_waitcnt vmcnt(%2) lgkmcnt(0)\n\ts_barrier" : "+v"(kaddr), "+v"(vb) : "i"(BV) : "memory"); \
    STAMP(5 + R);                                                                                         \
    f32x4 S[2][3], T[3];                                                                                  \
    u32x4 Pn[3], vf[3];                                                                                   \
    sfor<0, 51>([&](auto IC) __attribute__((always_inline)) {                                             \
      constexpr int i = IC.value;                                                                         \
      if constexpr (i < 3) T[i] = mfma(ones, P[i], f32x4{0, 0, 0, 0});                                    \
      else if constexpr (i < 27) {                                                                        \
        constexpr int q = i - 3, kb = q / 12, rt = (q % 12) / 4, ks = q % 4;                              \
        S[kb][rt] = mfma(kf[kb * 4 + ks], qf[rt][ks], ks ? S[kb][rt] : negm);                             \
      } else {                                                                                            \
        constexpr int q = i - 27, ps = q / 3, rt = q % 3;                                                 \
        if constexpr (R > 1) asm volatile("v_mfma_f32_16x16x32_bf16 %0, %1, %2, %0" : "+v"(O[ps][rt]) : "v"(vf[ps % 3]), "v"(P[rt])); \
        else asm volatile("v_mfma_f32_16x16x32_bf16 %0, %1, %2, 0" : "=&v"(O[ps][rt]) : "v"(vf[ps % 3]), "v"(P[rt])); \
      }                                                                                                   \
      if constexpr (i == DPOS(0)) { D0; }                                                                 \
      if constexpr (i == DPOS(1)) { D1; }                                                                 \
      if constexpr (i == DPOS(2)) { D2; }                                                                 \
      if constexpr (i == DPOS(3)) { D3; }                                                                 \
      if constexpr (i == DPOS(4)) { D4; }                                                                 \
      if constexpr (i == DPOS(5)) { D5; }                                                                 \
      if constexpr (i == DPOS(6)) { D6; }                                                                 \
      if constexpr (i == DPOS(7)) { D7; }                                                                 \
      if constexpr (i >= 4 && i < 7) { ls[i - 4] += T[i - 4][0]; PIN(ls[i - 4]); }                        \
      if constexpr (expAt(i) >= 0) {                                                                      \
        constexpr int e = expAt(i);                                                                       \
        S[e / 12][(e % 12) / 4][e % 4] = ex2(S[e / 12][(e % 12) / 4][e % 4]);                             \
      }                                                                                                   \
      if constexpr (cvtAt(i) >= 0) {                                                                      \
        constexpr int e = cvtAt(i);                                                                       \
        Pn[(e % 12) / 4][2 * (e / 12) + ((e % 4) >> 1)] = pk(S[e / 12][(e % 12) / 4][(e % 4) - 1], S[e / 12][(e % 12) / 4][e % 4]); \
      }                                                                                                   \
      if constexpr (i == 21) vf[0] = ldstr2(vb, vb + 4096);                                               \
      if constexpr (i == 24) vf[1] = ldstr2(vb + 256, vb + 256 + 4096);                                   \
      if constexpr (i >= 27 && (i - 27) % 3 == 0 && (i - 27) / 3 + 2 < 8) {                               \
        constexpr int pn = (i - 27) / 3 + 2;                                                              \
        vf[pn % 3] = ldstr2(vb + 256 * pn, vb + 256 * pn + 4096);                                         \
      }                                                                                                   \
      if constexpr (R < 7 && i == MUPOS) BARVN(MV >= 0 ? MV : 0);                                         \
      if constexpr (R < 7 && i > MUPOS && i <= MUPOS + 8) {                                               \
        constexpr int f = i - MUPOS - 1;                                                                  \
        kf[f] = lds128(kaddr + KS_(R + 1) + 4096 * (f >> 2) + 512 * (f & 3));                             \
      }                                                                                                   \
      if constexpr (i == 49) { vb += VS_(R) - VS_(R - 1); PIN(vb); }                                      \
      SB();                                                                                               \
    });                                                                                                   \
    _Pragma("unroll") for (int rt = 0; rt < 3; rt++) P[rt] = Pn[rt];                                      \
  }
#define NOP (void)0
  PHASE(1, 63, 8, IK(3, 0), IK(3, 1), IK(3, 2), IK(3, 3), NOP, NOP, NOP, NOP);
  PHASE(2, 63, 8, IK(4, 0), IK(4, 1), IK(4, 2), IK(4, 3), IV(3, 0), IV(3, 1), IV(3, 2), IV(3, 3));
  PHASE(3, 63, 12, IK(5, 0), IK(5, 1), IK(5, 2), IK(5, 3), IV(4, 0), IV(4, 1), IV(4, 2), IV(4, 3));
  PHASE(4, 8, 12, IK(6, 0), IK(6, 1), IK(6, 2), IK(6, 3), IV(5, 0), IV(5, 1), IV(5, 2), IV(5, 3));
  PHASE(5, 8, 12, IK(7, 0), IK(7, 1), IK(7, 2), IK(7, 3), IV(6, 0), IV(6, 1), IV(6, 2), IV(6, 3));
  RELANE();
  wlp = 1024u * ((lane >> 1) & 7) + 32u * g4 + 16u * (lane & 1);
  PHASE(6, 8, 12, IV(7, 0), IV(7, 1), IV(7, 2), IV(7, 3), IW(0), IW(1), IW(2), IW(3));
  PHASE(7, 8, -1, IW(4), IW(5), NOP, NOP, NOP, NOP, NOP, NOP);
  asm volatile("s_nop 7\n\ts_nop 7" ::: "memory");
  float mrow[3];
  bool bad;
  {
    bool bl = false;
#pragma unroll
    for (int rt = 0; rt < 3; rt++) {
      const f32x4 t = mfma(ones, P[rt], f32x4{0, 0, 0, 0});
      ls[rt] += t[0];
      float nm = negm[0];
      mrow[rt] = -nm;
      bl |= !(ls[rt] < 1073741824.0f) || !(ls[rt] >= 8.673617379884035e-19f);
    }
    bad = __any(bl);
  }
  if (bad) {
    RELANE();
    const u32 ko = l16 * 256u + 16u * g4, vo = g4 * 1024u + 2u * l16;
#pragma unroll
    for (int ps = 0; ps < 8; ps++)
#pragma unroll
      for (int rt = 0; rt < 3; rt++) O[ps][rt] = f32x4{0, 0, 0, 0};
#pragma unroll
    for (int rt = 0; rt < 3; rt++) { mrow[rt] = -3.0e38f; ls[rt] = 0.f; }
#pragma unroll 1
    for (int r = 0; r < 8; r++) {
      const unsigned char* Kr = Kg + (size_t)(r * 128 + q4 * 32) * 256;
      const unsigned char* Vr = Vg + (size_t)(r * 128 + q4 * 32) * 256;
      f32x4 S[2][3];
#pragma unroll
      for (int kb = 0; kb < 2; kb++) {
#pragma unroll
        for (int rt = 0; rt < 3; rt++) S[kb][rt] = f32x4{0, 0, 0, 0};
#pragma unroll
        for (int ks = 0; ks < 4; ks++) {
          u32x4 kx;
          GLD("", kx, ko, Kr + 4096 * kb, 64 * ks);
          asm volatile("s_waitcnt vmcnt(0)" : "+v"(kx)::"memory");
#pragma unroll
          for (int rt = 0; rt < 3; rt++) S[kb][rt] = mfma(kx, qf[rt][ks], S[kb][rt]);
        }
      }
      u32x4 P2[3];
#pragma unroll
      for (int rt = 0; rt < 3; rt++) {
        float m = fmaxf(fmaxf(fmaxf(S[0][rt][0], S[0][rt][1]), fmaxf(S[0][rt][2], S[0][rt][3])),
                        fmaxf(fmaxf(S[1][rt][0], S[1][rt][1]), fmaxf(S[1][rt][2], S[1][rt][3])));
        m = fmaxf(xmax(m), mrow[rt]);
        const float al = ex2(mrow[rt] - m);
        mrow[rt] = m;
        float e[8];
#pragma unroll
        for (int kb = 0; kb < 2; kb++)
#pragma unroll
          for (int jj = 0; jj < 4; jj++) e[kb * 4 + jj] = ex2(S[kb][rt][jj] - m);
        P2[rt] = u32x4{pk(e[0], e[1]), pk(e[2], e[3]), pk(e[4], e[5]), pk(e[6], e[7])};
        const f32x4 t = mfma(ones, P2[rt], f32x4{0, 0, 0, 0});
        ls[rt] = ls[rt] * al + t[0];
#pragma unroll
        for (int ps = 0; ps < 8; ps++) O[ps][rt] *= al;
      }
#pragma unroll
      for (int ps = 0; ps < 8; ps++) {
        const unsigned char* vp = Vr + 32 * (ps ^ (2 * q4));
        u32x4 vx;
#pragma unroll
        for (int jj = 0; jj < 4; jj++) {
          u32 lo_, hi_;
          asm volatile("global_load_ushort %0, %2, %3 offset:%4\n\tglobal_load_ushort %1, %2, %3 offset:%5\n\ts_waitcnt vmcnt(0)"
                       : "=&v"(lo_), "=&v"(hi_) : "v"(vo), "s"(vp + (jj >> 1) * 4096), "i"((jj & 1) * 512), "i"((jj & 1) * 512 + 256) : "memory");
          vx[jj] = lo_ | (hi_ << 16);
        }
#pragma unroll
        for (int rt = 0; rt < 3; rt++) O[ps][rt] = mfma(vx, P2[rt], O[ps][rt]);
      }
    }
#pragma unroll
    for (int rt = 0; rt < 3; rt++) P[rt] = u32x4{0, 0, 0, 0};
    WAITV(0);
  }
  RELANE();
  {
    const float mm = g4 == 0 ? mrow[0] : g4 == 1 ? mrow[1] : mrow[2];
    const float ll = g4 == 0 ? ls[0] : g4 == 1 ? ls[1] : ls[2];
    if (g4 < 3) st64(L0 + MLO + ((u32)(hd * 3) + g4) * 512u + l16 * 32u + 8u * q4, u32x2{__float_as_uint(mm), __float_as_uint(ll)});
  }
  BARV(6);
  STAMP(14); STCLK(22);
  vb = L0 + 8192u * q4 + 2048u * (g4 >> 1) + 128u * (g4 & 1) + 32u * ((lane >> 2) & 3) + 8u * (lane & 3) + VS_(7);
  float cw[3], lse[3];
  const u32 gw = (u32)((grp * 4 + j) * 8 + wave);
  {
    u32x4 vf[3];
    vf[0] = ldstr2(vb + 256 * 2, vb + 256 * 2 + 4096);
    vf[1] = ldstr2(vb + 256 * 3, vb + 256 * 3 + 4096);
    const u32 mr = L0 + MLO + (u32)(hd * 3) * 512u + l16 * 32u;
#pragma unroll
    for (int rt = 0; rt < 3; rt++) {
      const f32x4 a = lds128f(mr + rt * 512), c = lds128f(mr + rt * 512 + 16);
      const float M = fmaxf(fmaxf(a[0], a[2]), fmaxf(c[0], c[2]));
      const float Ls = (a[1] * ex2(a[0] - M) + a[3] * ex2(a[2] - M)) + (c[1] * ex2(c[0] - M) + c[3] * ex2(c[2] - M));
      cw[rt] = ex2(mrow[rt] - M) * __builtin_amdgcn_rcpf(Ls);
      lse[rt] = M + lg2(Ls);
    }
    if (g4 == 0) {
      u32x4 v = {__float_as_uint(lse[0]), __float_as_uint(lse[1]), __float_as_uint(lse[2]), tag32};
      __builtin_amdgcn_raw_buffer_store_b128(v, rs, WS_OL + gw * 256u + l16 * 16u, 0, 16);
    }
#pragma unroll
    for (int i = 0; i < 8; i++) {
      const int ps = (i + 2) & 7;
      if (i + 2 < 8) { const int pn = (i + 4) & 7; vf[(i + 2) % 3] = ldstr2(vb + 256 * pn, vb + 256 * pn + 4096); }
#pragma unroll
      for (int rt = 0; rt < 3; rt++) O[ps][rt] = mfma(vf[i % 3], P[rt], O[ps][rt]);
      if (i >= 2 && i <= 6 && (i & 1) == 0) {
        const int pi = i >> 1;
        const u32 mw = L0 + SCR + (u32)((hd * 4 + (q4 ^ pi)) * 9 + (pi - 1) * 3) * 1024u + lane * 16u;
#pragma unroll
        for (int rt = 0; rt < 3; rt++) {
          const f32x4 a = O[2 * pi][rt] * cw[rt], c = O[2 * pi + 1][rt] * cw[rt];
          st128(mw + rt * 1024, u32x4{pk(a[0], a[1]), pk(a[2], a[3]), pk(c[0], c[1]), pk(c[2], c[3])});
        }
      }
    }
  }
  BAR();
  u32x4 F[3];
  {
    const u32 mr = L0 + SCR + (u32)((hd * 4 + q4) * 9) * 1024u + lane * 16u;
#pragma unroll
    for (int rt = 0; rt < 3; rt++) {
      const u32x4 p0 = lds128(mr + rt * 1024), p1 = lds128(mr + (3 + rt) * 1024), p2 = lds128(mr + (6 + rt) * 1024);
      const f32x4 a = O[0][rt] * cw[rt], c = O[1][rt] * cw[rt];
      F[rt] = u32x4{pk(a[0] + (bflo(p0[0]) + bflo(p1[0]) + bflo(p2[0])), a[1] + (bfhi(p0[0]) + bfhi(p1[0]) + bfhi(p2[0]))),
                    pk(a[2] + (bflo(p0[1]) + bflo(p1[1]) + bflo(p2[1])), a[3] + (bfhi(p0[1]) + bfhi(p1[1]) + bfhi(p2[1]))),
                    pk(c[0] + (bflo(p0[2]) + bflo(p1[2]) + bflo(p2[2])), c[1] + (bfhi(p0[2]) + bfhi(p1[2]) + bfhi(p2[2]))),
                    pk(c[2] + (bflo(p0[3]) + bflo(p1[3]) + bflo(p2[3])), c[3] + (bfhi(p0[3]) + bfhi(p1[3]) + bfhi(p2[3])))};
    }
#pragma unroll
    for (int jj = 0; jj < 4; jj++) {
      u32x4 v = {F[0][jj], F[1][jj], F[2][jj], tag32};
      __builtin_amdgcn_raw_buffer_store_b128(v, rs, WS_OX + gw * 4096u + jj * 1024u + lane * 16u, 0, 16);
    }
    if (lane == 0) __hip_atomic_store((GAS u32*)(ws + WS_HA + (grp * 8 + wave) * 128) + j, tag32, __ATOMIC_RELAXED, __HIP_MEMORY_SCOPE_AGENT);
  }
  STAMP(15);
  const int cp = q4, k2 = hd;
  u32x4 wf[2][8];
  {
    const u32 wb = L0 + 2048u * (g4 >> 1) + 128u * (g4 & 1) + 32u * ((lane >> 2) & 3) + 8u * (lane & 3) + 65536u * k2;
    const u32 w0 = wb + 256u * (2 * cp + k2), w1 = wb + 256u * (2 * cp + (k2 ^ 1));
    BARV(6);
    IW(6); IW(7); IW(8); IW(9); IW(10); IW(11); IW(12); IW(13); IW(14); IW(15);
    if (k2 == 0) {
#pragma unroll
      for (int i = 0; i < 6; i++) { wf[0][i] = ldstr2(w0 + 8192 * i, w0 + 8192 * i + 4096); wf[1][i] = ldstr2(w1 + 8192 * i, w1 + 8192 * i + 4096); }
    }
    BARV(0);
    STAMP(16);
    if (k2 == 0) {
#pragma unroll
      for (int i = 6; i < 8; i++) { wf[0][i] = ldstr2(w0 + 8192 * i, w0 + 8192 * i + 4096); wf[1][i] = ldstr2(w1 + 8192 * i, w1 + 8192 * i + 4096); }
    } else {
#pragma unroll
      for (int i = 0; i < 8; i++) { wf[0][i] = ldstr2(w0 + 8192 * i, w0 + 8192 * i + 4096); wf[1][i] = ldstr2(w1 + 8192 * i, w1 + 8192 * i + 4096); }
    }
  }
  {
    {
      const unsigned char* hp_ = ws + WS_HA + (grp * 8 + wave) * 128;
      u32 n = 0;
      for (;;) {
        u32x4 hh;
        asm volatile("s_load_dwordx4 %0, %1, 0x0 glc\n\ts_waitcnt lgkmcnt(0)" : "=s"(hh) : "s"(hp_) : "memory");
        const u32 df = ((hh[0] ^ tag32) | (hh[1] ^ tag32)) | ((hh[2] ^ tag32) | (hh[3] ^ tag32));
        if (df == 0 || ++n > HINT_MAX) break;
        __builtin_amdgcn_s_sleep(1);
      }
      STVAL(30, n);
    }
    const u32 s1 = (u32)((grp * 4 + (j ^ 2)) * 8 + wave), s2 = (u32)((grp * 4 + (j ^ 1)) * 8 + wave), s3 = (u32)((grp * 4 + (j ^ 3)) * 8 + wave);
    const unsigned char* ox = ws + WS_OX;
    const unsigned char* ol = ws + WS_OL;
    const u32 vo = lane * 16u, lo = l16 * 16u;
    u32x4 G[3][4], Lg[3];
    u32 sp = 0;
    for (;;) {
      GLD(" sc1", Lg[0], lo, ol + s1 * 256u, 0); GLD(" sc1", Lg[1], lo, ol + s2 * 256u, 0); GLD(" sc1", Lg[2], lo, ol + s3 * 256u, 0);
#pragma unroll
      for (int jj = 0; jj < 4; jj++) {
        GLD(" sc1", G[0][jj], vo, ox + (size_t)s1 * 4096u, jj * 1024);
        GLD(" sc1", G[1][jj], vo, ox + (size_t)s2 * 4096u, jj * 1024);
        GLD(" sc1", G[2][jj], vo, ox + (size_t)s3 * 4096u, jj * 1024);
      }
      asm volatile("s_waitcnt vmcnt(0)"
                   : "+v"(G[0][0]), "+v"(G[0][1]), "+v"(G[0][2]), "+v"(G[0][3]), "+v"(G[1][0]), "+v"(G[1][1]), "+v"(G[1][2]), "+v"(G[1][3]),
                     "+v"(G[2][0]), "+v"(G[2][1]), "+v"(G[2][2]), "+v"(G[2][3]), "+v"(Lg[0]), "+v"(Lg[1]), "+v"(Lg[2])::"memory");
      bool ok = (Lg[0][3] == tag32) & (Lg[1][3] == tag32) & (Lg[2][3] == tag32);
#pragma unroll
      for (int i = 0; i < 3; i++)
#pragma unroll
        for (int jj = 0; jj < 4; jj++) ok &= (G[i][jj][3] == tag32);
      if (__all(ok)) break;
      if (++sp > SPIN_MAX) break;
    }
    STAMP(17);
    STVAL(29, sp);
    const u32 bo = L0 + (hp ? 0u : SC2) + (u32)(4 * hd + q4) * 3072u + lane * 16u;
    const u32 bt = L0 + (hp ? SC2 : 0u) + (u32)(4 * hd + q4) * 3072u + lane * 16u;
#pragma unroll
    for (int rt = 0; rt < 3; rt++) {
      {
        const float la = lse[rt], lb = __uint_as_float(Lg[0][rt]);
        const float M = fmaxf(la, lb);
        float ea = ex2(la - M), eb = ex2(lb - M);
        const float inv = __builtin_amdgcn_rcpf(ea + eb);
        ea *= inv; eb *= inv;
        u32x4 v;
#pragma unroll
        for (int jj = 0; jj < 4; jj++)
          v[jj] = pk(bflo(F[rt][jj]) * ea + bflo(G[0][jj][rt]) * eb, bfhi(F[rt][jj]) * ea + bfhi(G[0][jj][rt]) * eb);
        st128(bo + rt * 1024, v);
      }
      {
        const float la = __uint_as_float(Lg[1][rt]), lb = __uint_as_float(Lg[2][rt]);
        const float M = fmaxf(la, lb);
        float ea = ex2(la - M), eb = ex2(lb - M);
        const float inv = __builtin_amdgcn_rcpf(ea + eb);
        ea *= inv; eb *= inv;
        u32x4 v;
#pragma unroll
        for (int jj = 0; jj < 4; jj++)
          v[jj] = pk(bflo(G[1][jj][rt]) * ea + bflo(G[2][jj][rt]) * eb, bfhi(G[1][jj][rt]) * ea + bfhi(G[2][jj][rt]) * eb);
        st128(bt + rt * 1024, v);
      }
    }
  }
  BAR();
  STAMP(18);
  {
    const u32 bfr = L0 + (k2 ? 0u : SC2) + lane * 16u;
    f32x4 acc[2][3];
#pragma unroll
    for (int i = 0; i < 8; i++) {
      u32x4 Bf[3];
#pragma unroll
      for (int rt = 0; rt < 3; rt++) Bf[rt] = lds128(bfr + (u32)(i * 3 + rt) * 1024u);
#pragma unroll
      for (int c = 0; c < 2; c++)
#pragma unroll
        for (int rt = 0; rt < 3; rt++) acc[c][rt] = mfma(wf[c][i], Bf[rt], i ? acc[c][rt] : f32x4{0, 0, 0, 0});
    }
    const u32 rw = L0 + REDO + (u32)wave * 3072u + lane * 16u, rr = L0 + REDO + (u32)(wave ^ 4) * 3072u + lane * 16u;
#pragma unroll
    for (int rt = 0; rt < 3; rt++) st128f(rw + rt * 1024, acc[1][rt]);
    BAR();
    f32x4 r[3];
#pragma unroll
    for (int rt = 0; rt < 3; rt++) r[rt] = acc[0][rt] + lds128f(rr + rt * 1024);
    const int dst = 2 * cp + k2;
    const u32 po = WS_OP + (u32)(((b * 4 + j) * 8 + dst) * 16384 + kvh * 2048) + g4 * 512u + l16 * 16u;
#pragma unroll
    for (int e = 0; e < 2; e++) {
      u32x4 v = {pk(r[0][2 * e], r[0][2 * e + 1]), pk(r[1][2 * e], r[1][2 * e + 1]), pk(r[2][2 * e], r[2][2 * e + 1]), tag32};
      __builtin_amdgcn_raw_buffer_store_b128(v, rs, po + e * 256u, 0, 16);
    }
    if (lane == 0) __hip_atomic_store((GAS u32*)(ws + WS_HB + ((b * 4 + j) * 8 + dst) * 128) + kvh, tag32, __ATOMIC_RELAXED, __HIP_MEMORY_SCOPE_AGENT);
  }
  STAMP(19);
  RELANE();
  {
    {
      const unsigned char* hp_ = ws + WS_HB + ((b * 4 + j) * 8 + kvh) * 128;
      u32 n = 0;
      for (;;) {
        u32x8 hh;
        asm volatile("s_load_dwordx8 %0, %1, 0x0 glc\n\ts_waitcnt lgkmcnt(0)" : "=s"(hh) : "s"(hp_) : "memory");
        const u32 df = ((hh[0] ^ tag32) | (hh[1] ^ tag32) | (hh[2] ^ tag32) | (hh[3] ^ tag32)) |
                       ((hh[4] ^ tag32) | (hh[5] ^ tag32) | (hh[6] ^ tag32) | (hh[7] ^ tag32));
        if (df == 0 || ++n > HINT_MAX) break;
        __builtin_amdgcn_s_sleep(1);
      }
      STVAL(27, n);
    }
    STAMP(13);
    const int e = wave & 1, gq = wave >> 1;
    const unsigned char* pb = ws + WS_OP + (size_t)(((b * 4 + j) * 8 + kvh) * 16384 + gq * 512 + e * 256);
    const u32 vo = g4 * 2048u + l16 * 16u;
    u32x4 G[2];
    u32 sp = 0;
    for (;;) {
      GLD(" sc1", G[0], vo, pb, 0);
      GLD(" sc1", G[1], vo, pb + 8192, 0);
      asm volatile("s_waitcnt vmcnt(0)" : "+v"(G[0]), "+v"(G[1])::"memory");
      if (__all((G[0][3] == tag32) & (G[1][3] == tag32))) break;
      if (++sp > SPIN_MAX) break;
    }
    STVAL(28, sp);
    if (bid == b * 32 && wave == 0 && lane == 0) __hip_atomic_store((GAS u64*)(ws + WS_G + b * 128), tag, __ATOMIC_RELAXED, __HIP_MEMORY_SCOPE_AGENT);
    u32 res = 0;
#pragma unroll
    for (int rt = 0; rt < 3; rt++) {
      const float lo_ = xsum(bflo(G[0][rt]) + bflo(G[1][rt])), hi_ = xsum(bfhi(G[0][rt]) + bfhi(G[1][rt]));
      const u32 p = pk(lo_, hi_);
      if (g4 == (u32)rt) res = p;
    }
    const u32 row = 16u * g4 + l16, col = 128u * j + 16u * kvh + 4u * gq + 2u * e;
    if (g4 < 3) *(GAS u32*)(Out + (size_t)(b * 48 + row) * 512 + col) = res;
  }
  STAMP(20); STCLK(24);
}

extern "C" void kernel_launch(void* const* d_in, const int* in_sizes, int n_in, void* d_out, int out_size, void* d_ws, size_t ws_size,
                              hipStream_t stream) {
  (void)in_sizes; (void)n_in; (void)out_size; (void)ws_size;
  (void)hipFuncSetAttribute((const void*)gqa_fused, hipFuncAttributeMaxDynamicSharedMemorySize, 163840);
  gqa_fused<<<256, 512, 163840, stream>>>((const u16*)d_in[0], (const u16*)d_in[1], (const u16*)d_in[2], (const u16*)d_in[3],
                                          (const u16*)d_in[4], (u16*)d_out, (unsigned char*)d_ws);
}
```

```cpp
#include <hip/hip_runtime.h>
#include <stdint.h>

typedef unsigned int u32;
typedef unsigned long long u64;
typedef unsigned short u16;
typedef float f32x4 __attribute__((ext_vector_type(4)));
typedef float f32x2 __attribute__((ext_vector_type(2)));
typedef u32 u32x8 __attribute__((ext_vector_type(8)));
typedef u32 u32x4 __attribute__((ext_vector_type(4)));
typedef u32 u32x2 __attribute__((ext_vector_type(2)));
typedef __bf16 bf16x8 __attribute__((ext_vector_type(8)));
typedef __bf16 bf16x2 __attribute__((ext_vector_type(2)));
#define LAS __attribute__((address_space(3)))
#define GAS __attribute__((address_space(1)))
#define CAS __attribute__((address_space(4)))
#define DEVI __device__ __forceinline__

constexpr u32 WS_G = 0;
constexpr u32 WS_HB = 32768;
constexpr u32 WS_HA = 65536;
constexpr u32 WS_OL = 128u << 10;
constexpr u32 WS_OX = 1u << 20;
constexpr u32 WS_OP = 9u << 20;
constexpr u32 WS_END = 17u << 20;
static_assert(WS_HA + 64 * 8 * 128 <= WS_OL && WS_OL + 2048 * 384 <= WS_OX && WS_OX + 2048 * 4096 <= WS_OP, "workspace map");

constexpr u32 SA = 0, SBK = 32768, SC0 = 65536, SC1 = 98304, SC2 = 131072;
constexpr u32 XOFF = 98304;
constexpr u32 MLO = 49152;
constexpr u32 SCR = 57344;
constexpr u32 BFO = 98304;
constexpr u32 WL_(int h) { return h < 6 ? 8192u * h : h < 10 ? 131072u + 8192u * (h - 6) : 49152u + 8192u * (h - 10); }
constexpr u32 KS_(int n) { return (n & 1) ? SBK : SA; }
constexpr u32 TS_(int t) { return t % 3 == 0 ? SC0 : t % 3 == 1 ? SA : SBK; }
constexpr u32 VS_(int n) { return n % 3 == 0 ? SC0 : n % 3 == 1 ? SC2 : SC1; }

DEVI f32x4 mfma(u32x4 a, u32x4 b, f32x4 c) {
  return __builtin_amdgcn_mfma_f32_16x16x32_bf16(__builtin_bit_cast(bf16x8, a), __builtin_bit_cast(bf16x8, b), c, 0, 0, 0);
}
DEVI u32 pk(float a, float b) {
  f32x2 v = {a, b};
  return __builtin_bit_cast(u32, __builtin_convertvector(v, bf16x2));
}
DEVI float bflo(u32 v) { return __uint_as_float(v << 16); }
DEVI float bfhi(u32 v) { return __uint_as_float(v & 0xffff0000u); }
DEVI float addh(u32 v, u32 sel, float c) {
  return __builtin_amdgcn_fdot2_f32_bf16(__builtin_bit_cast(bf16x2, v), __builtin_bit_cast(bf16x2, sel), c, false);
}
DEVI u32x4 lds128(u32 a) { return *(const LAS u32x4*)(uintptr_t)a; }
DEVI f32x4 lds128f(u32 a) { return *(const LAS f32x4*)(uintptr_t)a; }
DEVI void st128(u32 a, u32x4 v) { *(LAS u32x4*)(uintptr_t)a = v; }
DEVI void st128f(u32 a, f32x4 v) { *(LAS f32x4*)(uintptr_t)a = v; }
DEVI void st64(u32 a, u32x2 v) { *(LAS u32x2*)(uintptr_t)a = v; }
DEVI void st64f(u32 a, f32x2 v) { *(LAS f32x2*)(uintptr_t)a = v; }
DEVI f32x2 lds64f(u32 a) { return *(const LAS f32x2*)(uintptr_t)a; }
DEVI u32x2 ldstr(u32 a) {
  typedef short v4i16 __attribute__((ext_vector_type(4)));
  return __builtin_bit_cast(u32x2, __builtin_amdgcn_ds_read_tr16_b64_v4i16((LAS v4i16*)(uintptr_t)a));
}
DEVI u32x4 ldstr2(u32 a0, u32 a1) {
  u32x2 x = ldstr(a0), y = ldstr(a1);
  u32x4 r = {x[0], x[1], y[0], y[1]};
  return r;
}
DEVI float ex2(float x) { return __builtin_amdgcn_exp2f(x); }
DEVI float lg2(float x) { return __builtin_amdgcn_logf(x); }
DEVI float xmax(float v) {
  auto r = __builtin_amdgcn_permlane16_swap(__float_as_uint(v), __float_as_uint(v), false, false);
  v = fmaxf(__uint_as_float(r[0]), __uint_as_float(r[1]));
  auto q = __builtin_amdgcn_permlane32_swap(__float_as_uint(v), __float_as_uint(v), false, false);
  return fmaxf(__uint_as_float(q[0]), __uint_as_float(q[1]));
}
DEVI void bcast3(float v, float* o) {
  auto r = __builtin_amdgcn_permlane16_swap(__float_as_uint(v), __float_as_uint(v), false, false);
  auto e = __builtin_amdgcn_permlane32_swap(r[0], r[0], false, false);
  auto d = __builtin_amdgcn_permlane32_swap(r[1], r[1], false, false);
  o[0] = __uint_as_float(e[0]); o[1] = __uint_as_float(d[0]); o[2] = __uint_as_float(e[1]);
}
DEVI float xsum(float v) {
  auto r = __builtin_amdgcn_permlane16_swap(__float_as_uint(v), __float_as_uint(v), false, false);
  v = __uint_as_float(r[0]) + __uint_as_float(r[1]);
  auto q = __builtin_amdgcn_permlane32_swap(__float_as_uint(v), __float_as_uint(v), false, false);
  return __uint_as_float(q[0]) + __uint_as_float(q[1]);
}

#define KPOL " nt"
#define VPOL " nt"
#define WPOL ""
#define PPOL ""
#define QPOL ""
#define XPOL ""
#define SAUX 16
#define GPOL " sc1"
#define DMA1(POL, M, VO, SB_, OFF)                                                               \
  asm volatile("s_mov_b32 m0, %0\n\ts_nop 0\n\tglobal_load_lds_dwordx4 %1, %2 offset:%3" POL ::"s"(M), "v"(VO), "s"(SB_), "i"(OFF) : "memory")
#define GLD(POL, D, VO, SB_, OFF)                                                                \
  asm volatile("global_load_dwordx4 %0, %1, %2 offset:%3" POL : "=v"(D) : "v"(VO), "s"(SB_), "i"(OFF) : "memory")
#define GLD2(POL, D, VO, SB_) asm volatile("global_load_dwordx2 %0, %1, %2" POL : "=v"(D) : "v"(VO), "s"(SB_) : "memory")
#define WAITV(N) asm volatile("s_waitcnt vmcnt(%0)" ::"i"(N) : "memory")
#define WAITL() asm volatile("s_waitcnt lgkmcnt(0)" ::: "memory")
#define PIN(x) asm volatile("" : "+v"(x))
#define SB() __builtin_amdgcn_sched_barrier(0)
#define RELANE()                                                                                                   \
  do {                                                                                                             \
    asm volatile("v_mbcnt_lo_u32_b32 %0, -1, 0\n\tv_mbcnt_hi_u32_b32 %0, -1, %0" : "=v"(lane));                    \
    l16 = lane & 15u; g4 = lane >> 4;                                                                              \
  } while (0)
#define KORD 1
#define USE_RBAR 1
#define RBAR() asm volatile("s_barrier" ::: "memory")
#define BAR() asm volatile("s_waitcnt lgkmcnt(0)\n\ts_barrier" ::: "memory")
#define BARV(N) asm volatile("s_waitcnt vmcnt(%0) lgkmcnt(0)\n\ts_barrier" ::"i"(N) : "memory")
#define BARVN(N) asm volatile("s_waitcnt vmcnt(%0)\n\ts_barrier" ::"i"(N) : "memory")

template <int V> struct IC_ { static constexpr int value = V; };
template <int I, int N, class F> DEVI void sfor(F&& f) {
  if constexpr (I < N) { f(IC_<I>{}); sfor<I + 1, N>(f); }
}
#define EXP0 8
#define EXPN 11
#define MUPOS 39
constexpr int expAt(int i) { for (int e = 0; e < 24; e++) if (EXP0 + (e * EXPN) / 8 == i) return e; return -1; }
constexpr int cvtAt(int i) { for (int e = 1; e < 24; e += 2) if (EXP0 + 2 + (e * EXPN) / 8 == i) return e; return -1; }
#define DP0 1
#define DPS 5
constexpr int DPOS(int d) { return DP0 + DPS * d; }
static_assert(DPOS(7) < MUPOS, "all DMA statements precede the mid-phase wait");

constexpr u32 SPIN_MAX = 1u << 14;
constexpr u32 HINT_MAX = 4000;

__global__ __launch_bounds__(512, 2) void gqa_fused(const u16* __restrict__ X, const u16* __restrict__ Kc, const u16* __restrict__ Vc,
                                                    const u16* __restrict__ Wq, const u16* __restrict__ Wo, u16* __restrict__ Out,
                                                    unsigned char* __restrict__ ws) {
  extern __shared__ __attribute__((aligned(16))) unsigned char smem[];
  const int wave = __builtin_amdgcn_readfirstlane(threadIdx.x >> 6);
  u32 lane, l16, g4;
  RELANE();
  const int q4 = wave & 3, hd = wave >> 2;
  const int bid = blockIdx.x;
  const int kvh = bid & 7, j = (bid >> 3) & 3, b = bid >> 5;
  const int hp = j & 1, kh = j >> 1;
  const int grp = b * 8 + kvh;
  const u32 L0 = (u32)(uintptr_t)(LAS unsigned char*)smem;
  const u64 tag = *(const CAS u64*)(uintptr_t)(ws + WS_G + b * 128) + 1ull;
  const u32 tag32 = (u32)tag;
  const __amdgpu_buffer_rsrc_t rs = __builtin_amdgcn_make_buffer_rsrc(ws, 0, WS_END, 0x00020000);
  const u32 dx = lane >> 3, dsl = (lane & 7u) ^ (dx & 6u);
  const u32 kvo = 4096u * wave + 256u * dx + 16u * dsl;
  const u32 vvo = 4096u * wave + 256u * dx + 16u * (dsl ^ (u32)(4 * ((wave >> 1) & 1)));
  const int kvrow = grp * 2048 + kh * 1024;
  const unsigned char* Kg = (const unsigned char*)Kc + (size_t)kvrow * 256;
  const unsigned char* Vg = (const unsigned char*)Vc + (size_t)kvrow * 256;
  const unsigned char* Vg0 = Vg + 128 * hd;
  const unsigned char* Vg1 = Vg + 128 * (hd ^ 1) - 1024;
  const u32 Lw = L0 + 4096u * wave;
#define IK(r, p) DMA1(KPOL, Lw + KS_(r), kvo, Kg + (r) * 32768 - (((p) & 1) ? 896 : 0), 1024 * (p))
#define IV(r, p) DMA1(VPOL, Lw + VS_(r), vvo, (((p) & 1) ? Vg1 : Vg0) + (r) * 32768, 1024 * (p))
#define IK4(r) do { IK(r, 0); IK(r, 1); IK(r, 2); IK(r, 3); } while (0)
#define IV4(r) do { IV(r, 0); IV(r, 1); IV(r, 2); IV(r, 3); } while (0)

  const unsigned char* Wog = (const unsigned char*)Wo + (size_t)(kvh * 512 + hp * 256 + 8 * (wave >> 1)) * 1024 + 512 * kh + 128 * (wave & 1);
  u32x4 kf[8];
  u32x4 qf[3][4];
  u32 kaddr = L0 + 8192u * q4 + 2048u * (l16 >> 3) + 128u * (l16 & 7) + 16u * (g4 ^ (l16 & 6));
#define KFRAG(slot, f) lds128(((f) & 1 ? kaddr ^ 64u : kaddr) + (slot) + 4096 * ((f) >> 2) + 1024 * (((f) >> 1) & 1))
  {
    const int cq = q4, k2 = hd;
    const u32 wqe = 8192u * dx + 16u * dsl, wqd = wqe ^ 64u;
    const unsigned char* wqb = (const unsigned char*)Wq + (size_t)(k2 * 256) * 8192 + ((4 * kvh + 2 * hp) * 128 + 64 * cq) * 2;
    auto issueWq = [&](int t) {
      const unsigned char* sb = wqb + (size_t)((t + b) & 7) * (32 * 8192);
      DMA1(QPOL, Lw + TS_(t), wqe, sb, 0);
      DMA1(QPOL, Lw + TS_(t), wqd, sb + 65536 - 1024, 1024);
      DMA1(QPOL, Lw + TS_(t), wqe, sb + 2 * 65536 - 2048, 2048);
      DMA1(QPOL, Lw + TS_(t), wqd, sb + 3 * 65536 - 3072, 3072);
    };
    {
      const u32 xo = 1024u * dx + 16u * dsl;
      const unsigned char* xb = (const unsigned char*)X + (size_t)(48 * b) * 1024 + wave * 128;
#pragma unroll
      for (int p = 0; p < 6; p++) DMA1(XPOL, L0 + XOFF + 6144u * wave + 1024u * p, xo, xb + p * 8192, 0);
    }
    DMA1(KPOL, Lw + SBK, kvo, Kg, 0); DMA1(KPOL, Lw + SBK, kvo, Kg - 896, 1024);
    DMA1(KPOL, Lw + SBK, kvo, Kg, 2048); DMA1(KPOL, Lw + SBK, kvo, Kg - 896, 3072);
    issueWq(0); issueWq(1);
    f32x4 qa[4][3];
    const u32 ab = Lw + 1024u * g4 + 128u * ((lane >> 2) & 3) + 32u * (((lane >> 3) & 1) ^ (2u * (g4 & 1)) ^ (u32)(2 * k2)) + 8u * (lane & 3);
    const u32 xbase = L0 + XOFF + 24576u * k2 + 1024u * (l16 >> 3) + 128u * (l16 & 7) + 16u * (g4 ^ (l16 & 6));
    auto qstep = [&](int t) {
      const int kq = (t + b) & 7;
      const u32 xa = (xbase ^ (u32)(64 * (kq & 1))) + (u32)(6144 * (kq >> 1));
      u32x4 a[4], xf[3];
#pragma unroll
      for (int c = 0; c < 4; c++) a[c] = ldstr2((ab ^ (u32)(32 * c)) + TS_(t), (ab ^ (u32)(32 * (c ^ 2))) + TS_(t) + 512);
#pragma unroll
      for (int rt = 0; rt < 3; rt++) xf[rt] = lds128(xa + 2048 * rt);
#pragma unroll
      for (int c = 0; c < 4; c++)
#pragma unroll
        for (int rt = 0; rt < 3; rt++) qa[c][rt] = mfma(a[c], xf[rt], t ? qa[c][rt] : f32x4{0, 0, 0, 0});
      WAITL();
    };
    BARVN(4);
#pragma unroll
    for (int f = 0; f < 8; f++) { kf[f] = KFRAG(SBK, f); }
    BAR();
#pragma unroll
    for (int f = 0; f < 8; f++) { PIN(kf[f]); }
    issueWq(2);
    qstep(0); issueWq(3); WAITV(8);
    qstep(1); issueWq(4); WAITV(8);
    qstep(2); issueWq(5); WAITV(8);
    qstep(3); issueWq(6); WAITV(8);
    qstep(4); issueWq(7);
    if (USE_RBAR) RBAR();
    WAITV(8);
    qstep(5); IK4(1); WAITV(8);
    qstep(6); WAITV(4);
    qstep(7);
    BAR();
    IV4(0);
    {
      const u32 sw = L0 + XOFF + 6144u * wave + lane * 16u;
#pragma unroll
      for (int i = 0; i < 6; i++) st128f(sw + 1024 * i, qa[2 + i / 3][i % 3]);
    }
    BAR();
    {
      const u32 pr = L0 + XOFF + 6144u * (wave ^ 4);
      f32x4 o[6];
#pragma unroll
      for (int i = 0; i < 6; i++) o[i] = lds128f(pr + lane * 16u + 1024 * i);
      const float c = 0.08838834764831845f * 1.4426950408889634f;
      u32x2 qo[6];
#pragma unroll
      for (int i = 0; i < 6; i++) {
        const f32x4 q = (qa[i / 3][i % 3] + o[i]) * c;
        qo[i] = u32x2{pk(q[0], q[1]), pk(q[2], q[3])};
      }
      WAITL();
      const u32 qw = pr + 256u * (g4 >> 1) + 16u * l16 + 8u * (g4 & 1);
#pragma unroll
      for (int i = 0; i < 6; i++) st64(qw + 1024 * (i % 3) + 512 * (i / 3), qo[i]);
    }
    BAR();
    IK4(2);
    const u32 qr = L0 + XOFF + 12288u * hd + 16u * lane;
#pragma unroll
    for (int ks = 0; ks < 4; ks++)
#pragma unroll
      for (int rt = 0; rt < 3; rt++) qf[rt][ks] = lds128(qr + 6144 * (ks >> 1) + 24576 * ((ks & 1) ^ 1) + 1024 * rt);
  }
#define VBASE(n) (L0 + VS_(n) + 8192u * q4 + 2048u * (g4 >> 1) + 512u * (g4 & 1) + 128u * ((lane >> 2) & 3) + 32u * (2u * (g4 & 1) + ((lane >> 3) & 1)) + 8u * (lane & 3))
  u32 vb = VBASE(0);
#define VFRAG(ps) ldstr2((vb ^ (32u * ((ps) & 3))) + 1024 * ((ps) >> 2), (vb ^ (32u * ((ps) & 3))) + 1024 * ((ps) >> 2) + 4096)
  f32x4 O[8][3];
  f32x4 negm;
  float ls[3] = {0.f, 0.f, 0.f};
  u32x4 P[3];
  u32x4 ones = {0x3f803f80u, 0x3f803f80u, 0x3f803f80u, 0x3f803f80u};
  PIN(ones);
  u32x4 pf0, pf1;
#define PFDONE do { PIN(pf0); PIN(pf1); } while (0)
  {
    f32x4 S0[3], S1[3];
#pragma unroll
    for (int rt = 0; rt < 3; rt++)
#pragma unroll
      for (int ks = 0; ks < 4; ks++) S0[rt] = mfma(kf[ks], qf[rt][ks], ks ? S0[rt] : f32x4{0, 0, 0, 0});
#pragma unroll
    for (int rt = 0; rt < 3; rt++)
#pragma unroll
      for (int ks = 0; ks < 4; ks++) S1[rt] = mfma(kf[4 + ks], qf[rt][ks], ks ? S1[rt] : f32x4{0, 0, 0, 0});
    SB();
    BARV(8);
    IV4(1);
    { const u32 xo = kvo - 4096u * wave + 768u * dx; GLD(PPOL, pf0, xo, Wog + b * 32768, 0); GLD(PPOL, pf1, xo, Wog + b * 32768, 256); }
#pragma unroll
    for (int f = 0; f < 8; f++) kf[f] = KFRAG(KS_(1), f);
    float m = -3.0e38f;
#pragma unroll
    for (int rt = 0; rt < 3; rt++)
      m = fmaxf(m, fmaxf(fmaxf(fmaxf(S0[rt][0], S0[rt][1]), fmaxf(S0[rt][2], S0[rt][3])),
                         fmaxf(fmaxf(S1[rt][0], S1[rt][1]), fmaxf(S1[rt][2], S1[rt][3]))));
    m = xmax(m);
    negm = f32x4{-m, -m, -m, -m};
#pragma unroll
    for (int rt = 0; rt < 3; rt++) {
      P[rt] = u32x4{pk(ex2(S0[rt][0] - m), ex2(S0[rt][1] - m)), pk(ex2(S0[rt][2] - m), ex2(S0[rt][3] - m)),
                    pk(ex2(S1[rt][0] - m), ex2(S1[rt][1] - m)), pk(ex2(S1[rt][2] - m), ex2(S1[rt][3] - m))};
    }
  }
  u32 wlp = 0;
#define IW(u) DMA1(WPOL, L0 + 1024u * wave + WL_(u), wlp, Wog + ((u) >> 1) * 32768 + ((u) & 1) * 256, 0)
#define PHASE(R, BV, MV, D0, D1, D2, D3, D4, D5, D6, D7, EX, PB)                                                  \
  {                                                                                                       \
    asm volatile("s_waitcnt vmcnt(%2) lgkmcnt(0)\n\ts_barrier" : "+v"(kaddr), "+v"(vb) : "i"(BV) : "memory"); \
    PB;                                                                                                   \
    f32x4 S[2][3], T[3];                                                                                  \
    u32x4 Pn[3], vf[3];                                                                                   \
    sfor<0, 51>([&](auto IC) __attribute__((always_inline)) {                                             \
      constexpr int i = IC.value;                                                                         \
      if constexpr (i < 3) T[i] = mfma(ones, P[i], f32x4{0, 0, 0, 0});                                    \
      else if constexpr (i < 27) {                                                                        \
        constexpr int q = i - 3, kb = q / 12, rt = (q % 12) / 4, ks = q % 4;                              \
        S[kb][rt] = mfma(kf[kb * 4 + ks], qf[rt][ks], ks ? S[kb][rt] : negm);                             \
      } else {                                                                                            \
        constexpr int q = i - 27, ps = q / 3, rt = q % 3;                                                 \
        if constexpr (R > 1) asm volatile("v_mfma_f32_16x16x32_bf16 %0, %1, %2, %0" : "+v"(O[ps][rt]) : "v"(vf[ps % 3]), "v"(P[rt])); \
        else asm volatile("v_mfma_f32_16x16x32_bf16 %0, %1, %2, 0" : "=&v"(O[ps][rt]) : "v"(vf[ps % 3]), "v"(P[rt])); \
      }                                                                                                   \
      if constexpr (i == DPOS(0)) { D0; }                                                                 \
      if constexpr (i == DPOS(1)) { D1; }                                                                 \
      if constexpr (i == DPOS(2)) { D2; }                                                                 \
      if constexpr (i == DPOS(3)) { D3; }                                                                 \
      if constexpr (i == DPOS(4)) { D4; }                                                                 \
      if constexpr (i == DPOS(5)) { D5; }                                                                 \
      if constexpr (i == DPOS(6)) { D6; }                                                                 \
      if constexpr (i == DPOS(7)) { D7; }                                                                 \
      if constexpr (i == 48) { EX; }                                                                      \
      if constexpr (i >= 4 && i < 7) { ls[i - 4] += T[i - 4][0]; PIN(ls[i - 4]); }                        \
      if constexpr (expAt(i) >= 0) {                                                                      \
        constexpr int e = expAt(i);                                                                       \
        S[e / 12][(e % 12) / 4][e % 4] = ex2(S[e / 12][(e % 12) / 4][e % 4]);                             \
      }                                                                                                   \
      if constexpr (cvtAt(i) >= 0) {                                                                      \
        constexpr int e = cvtAt(i);                                                                       \
        Pn[(e % 12) / 4][2 * (e / 12) + ((e % 4) >> 1)] = pk(S[e / 12][(e % 12) / 4][(e % 4) - 1], S[e / 12][(e % 12) / 4][e % 4]); \
      }                                                                                                   \
      if constexpr (i == 21) vf[0] = VFRAG(0);                                                            \
      if constexpr (i == 24) vf[1] = VFRAG(1);                                                            \
      if constexpr (i >= 27 && (i - 27) % 3 == 0 && (i - 27) / 3 + 2 < 8) {                               \
        constexpr int pn = (i - 27) / 3 + 2;                                                              \
        vf[pn % 3] = VFRAG(pn);                                                                           \
      }                                                                                                   \
      if constexpr (R < 7 && i == MUPOS) BARVN(MV >= 0 ? MV : 0);                                         \
      if constexpr (R < 7 && i > MUPOS && i <= MUPOS + 8) {                                               \
        constexpr int f = i - MUPOS - 1;                                                                  \
        kf[f] = KFRAG(KS_(R + 1), f);                                                                     \
      }                                                                                                   \
      if constexpr (i == 49) { vb += VS_(R) - VS_(R - 1); PIN(vb); }                                      \
      SB();                                                                                               \
    });                                                                                                   \
    _Pragma("unroll") for (int rt = 0; rt < 3; rt++) P[rt] = Pn[rt];                                      \
  }
#define NOP (void)0
  PHASE(1, 8, 12, IK(3, 0), IK(3, 1), IK(3, 2), IK(3, 3), IV(2, 0), IV(2, 1), IV(2, 2), IV(2, 3), NOP, NOP);
  PHASE(2, 8, 12, IK(4, 0), IK(4, 1), IK(4, 2), IK(4, 3), IV(3, 0), IV(3, 1), IV(3, 2), IV(3, 3), NOP, PFDONE);
  RELANE();
  wlp = 1024u * (lane >> 3) + 16u * ((lane & 7u) ^ ((lane >> 3) & 6u));
  PHASE(3, 8, 12, IK(5, 0), IK(5, 1), IK(5, 2), IK(5, 3), IV(4, 0), IV(4, 1), IV(4, 2), IV(4, 3), NOP, NOP);
  PHASE(4, 8, 12, IK(6, 0), IK(6, 1), IK(6, 2), IK(6, 3), IV(5, 0), IV(5, 1), IV(5, 2), IV(5, 3), NOP, NOP);
  PHASE(5, 8, 12, IK(7, 0), IK(7, 1), IK(7, 2), IK(7, 3), IV(6, 0), IV(6, 1), IV(6, 2), IV(6, 3), NOP, NOP);
  PHASE(6, 8, 12, IV(7, 0), IV(7, 1), IV(7, 2), IV(7, 3), IW(0), IW(1), IW(2), IW(3), NOP, NOP);
  PHASE(7, 8, -1, IW(4), IW(5), NOP, NOP, NOP, NOP, NOP, NOP, NOP, NOP);
  asm volatile("s_nop 7\n\ts_nop 7" ::: "memory");
  float mrow[3];
  bool bad;
  {
    bool bl = false;
#pragma unroll
    for (int rt = 0; rt < 3; rt++) {
      const f32x4 t = mfma(ones, P[rt], f32x4{0, 0, 0, 0});
      ls[rt] += t[0];
      float nm = negm[0];
      mrow[rt] = -nm;
      bl |= !(ls[rt] < 1073741824.0f) || !(ls[rt] >= 8.673617379884035e-19f);
    }
    bad = __any(bl);
  }
  if (bad) {
    RELANE();
    const u32 ko = l16 * 256u + 16u * g4, vo = g4 * 1024u + 2u * l16;
#pragma unroll
    for (int ps = 0; ps < 8; ps++)
#pragma unroll
      for (int rt = 0; rt < 3; rt++) O[ps][rt] = f32x4{0, 0, 0, 0};
#pragma unroll
    for (int rt = 0; rt < 3; rt++) { mrow[rt] = -3.0e38f; ls[rt] = 0.f; }
#pragma unroll 1
    for (int r = 0; r < 8; r++) {
      const unsigned char* Kr = Kg + (size_t)(r * 128 + q4 * 32) * 256;
      const unsigned char* Vr = Vg + (size_t)(r * 128 + q4 * 32) * 256;
      f32x4 S[2][3];
#pragma unroll
      for (int kb = 0; kb < 2; kb++) {
#pragma unroll
        for (int rt = 0; rt < 3; rt++) S[kb][rt] = f32x4{0, 0, 0, 0};
#pragma unroll
        for (int ks = 0; ks < 4; ks++) {
          u32x4 kx;
          GLD("", kx, ko, Kr + 4096 * kb, 64 * ks);
          asm volatile("s_waitcnt vmcnt(0)" : "+v"(kx)::"memory");
#pragma unroll
          for (int rt = 0; rt < 3; rt++) S[kb][rt] = mfma(kx, qf[rt][ks], S[kb][rt]);
        }
      }
      u32x4 P2[3];
#pragma unroll
      for (int rt = 0; rt < 3; rt++) {
        float m = fmaxf(fmaxf(fmaxf(S[0][rt][0], S[0][rt][1]), fmaxf(S[0][rt][2], S[0][rt][3])),
                        fmaxf(fmaxf(S[1][rt][0], S[1][rt][1]), fmaxf(S[1][rt][2], S[1][rt][3])));
        m = fmaxf(xmax(m), mrow[rt]);
        const float al = ex2(mrow[rt] - m);
        mrow[rt] = m;
        float e[8];
#pragma unroll
        for (int kb = 0; kb < 2; kb++)
#pragma unroll
          for (int jj = 0; jj < 4; jj++) e[kb * 4 + jj] = ex2(S[kb][rt][jj] - m);
        P2[rt] = u32x4{pk(e[0], e[1]), pk(e[2], e[3]), pk(e[4], e[5]), pk(e[6], e[7])};
        const f32x4 t = mfma(ones, P2[rt], f32x4{0, 0, 0, 0});
        ls[rt] = ls[rt] * al + t[0];
#pragma unroll
        for (int ps = 0; ps < 8; ps++) O[ps][rt] *= al;
      }
#pragma unroll
      for (int ps = 0; ps < 8; ps++) {
        const unsigned char* vp = Vr + 32 * (ps ^ (2 * q4));
        u32x4 vx;
#pragma unroll
        for (int jj = 0; jj < 4; jj++) {
          u32 lo_, hi_;
          asm volatile("global_load_ushort %0, %2, %3 offset:%4\n\tglobal_load_ushort %1, %2, %3 offset:%5\n\ts_waitcnt vmcnt(0)"
                       : "=&v"(lo_), "=&v"(hi_) : "v"(vo), "s"(vp + (jj >> 1) * 4096), "i"((jj & 1) * 512), "i"((jj & 1) * 512 + 256) : "memory");
          vx[jj] = lo_ | (hi_ << 16);
        }
#pragma unroll
        for (int rt = 0; rt < 3; rt++) O[ps][rt] = mfma(vx, P2[rt], O[ps][rt]);
      }
    }
#pragma unroll
    for (int rt = 0; rt < 3; rt++) P[rt] = u32x4{0, 0, 0, 0};
    WAITV(0);
  }
  RELANE();
  const float mm = g4 == 0 ? mrow[0] : g4 == 1 ? mrow[1] : mrow[2];
  {
    const float ll = g4 == 0 ? ls[0] : g4 == 1 ? ls[1] : ls[2];
    if (g4 < 3) st64(L0 + MLO + ((u32)(hd * 3) + g4) * 512u + l16 * 32u + 8u * q4, u32x2{__float_as_uint(mm), __float_as_uint(ll)});
  }
  BARV(6);
  vb = VBASE(7);
  float cw[3], lse;
  const u32 rtl = g4 < 3 ? g4 : 2u;
  const u32 gw = (u32)((grp * 4 + j) * 8 + wave);
  {
    u32x4 vf[3];
    vf[0] = VFRAG(2);
    vf[1] = VFRAG(3);
    {
      const u32 mr = L0 + MLO + ((u32)(hd * 3) + rtl) * 512u + l16 * 32u;
      const f32x4 a = lds128f(mr), c = lds128f(mr + 16);
      const float M = fmaxf(fmaxf(a[0], a[2]), fmaxf(c[0], c[2]));
      const float Ls = (a[1] * ex2(a[0] - M) + a[3] * ex2(a[2] - M)) + (c[1] * ex2(c[0] - M) + c[3] * ex2(c[2] - M));
      bcast3(ex2(mm - M) * __builtin_amdgcn_rcpf(Ls), cw);
      lse = M + lg2(Ls);
      if (g4 < 3) {
        u32x2 v = {__float_as_uint(lse), tag32};
        __builtin_amdgcn_raw_buffer_store_b64(v, rs, WS_OL + gw * 384u + g4 * 128u + l16 * 8u, 0, SAUX);
      }
    }
#pragma unroll
    for (int i = 0; i < 8; i++) {
      const int ps = (i + 2) & 7;
      if (i + 2 < 8) { const int pn = (i + 4) & 7; vf[(i + 2) % 3] = VFRAG(pn); }
#pragma unroll
      for (int rt = 0; rt < 3; rt++) O[ps][rt] = mfma(vf[i % 3], P[rt], O[ps][rt]);
      if (i >= 2 && i <= 6 && (i & 1) == 0) {
        const int pi = i >> 1;
        const u32 mw = L0 + SCR + (u32)((hd * 4 + (q4 ^ pi)) * 9 + (pi - 1) * 3) * 1024u + lane * 16u;
#pragma unroll
        for (int rt = 0; rt < 3; rt++) {
          const f32x4 a = O[2 * pi][rt] * cw[rt], c = O[2 * pi + 1][rt] * cw[rt];
          st128(mw + rt * 1024, u32x4{pk(a[0], a[1]), pk(a[2], a[3]), pk(c[0], c[1]), pk(c[2], c[3])});
        }
      }
    }
  }
  BAR();
  IW(6); IW(7); IW(8); IW(9);
  u32x4 F[3];
  float Ff[3][8];
  u32 slo = 0x00003f80u, shi = 0x3f800000u;
  asm volatile("" : "+s"(slo), "+s"(shi));
  {
    const u32 mr = L0 + SCR + (u32)((hd * 4 + q4) * 9) * 1024u + lane * 16u;
#pragma unroll
    for (int rt = 0; rt < 3; rt++) {
      const u32x4 p0 = lds128(mr + rt * 1024), p1 = lds128(mr + (3 + rt) * 1024), p2 = lds128(mr + (6 + rt) * 1024);
      const f32x4 a = O[0][rt] * cw[rt], c = O[1][rt] * cw[rt];
#pragma unroll
      for (int jj = 0; jj < 4; jj++) {
        const float x0 = jj < 2 ? a[2 * jj] : c[2 * jj - 4], x1 = jj < 2 ? a[2 * jj + 1] : c[2 * jj - 3];
        Ff[rt][2 * jj] = addh(p2[jj], slo, addh(p1[jj], slo, addh(p0[jj], slo, x0)));
        Ff[rt][2 * jj + 1] = addh(p2[jj], shi, addh(p1[jj], shi, addh(p0[jj], shi, x1)));
        F[rt][jj] = pk(Ff[rt][2 * jj], Ff[rt][2 * jj + 1]);
      }
    }
#pragma unroll
    for (int jj = 0; jj < 4; jj++) {
      u32x4 v = {F[0][jj], F[1][jj], F[2][jj], tag32};
      __builtin_amdgcn_raw_buffer_store_b128(v, rs, WS_OX + gw * 4096u + jj * 1024u + lane * 16u, 0, SAUX);
    }
    if (lane == 0) __hip_atomic_store((GAS u32*)(ws + WS_HA + (grp * 8 + wave) * 128) + j, tag32, __ATOMIC_RELAXED, __HIP_MEMORY_SCOPE_AGENT);
  }
  u32x4 wf[2][8];
  {
    const u32 wb = L0 + 2048u * (g4 >> 1) + 512u * (g4 & 1) + 128u * ((lane >> 2) & 3) + 32u * (2u * (g4 & 1) + ((lane >> 3) & 1)) + 8u * (lane & 3) + 8192u * hd;
    const int c0 = 2 * q4, c1 = 2 * q4 + 1;
    const u32 w0 = (wb ^ (u32)(32 * (c0 & 3))) + 1024u * (c0 >> 2), w1 = (wb ^ (u32)(32 * (c1 & 3))) + 1024u * (c1 >> 2);
#define WFRAG(i) do { wf[0][i] = ldstr2(w0 + WL_(2 * (i)), w0 + WL_(2 * (i)) + 4096); wf[1][i] = ldstr2(w1 + WL_(2 * (i)), w1 + WL_(2 * (i)) + 4096); } while (0)
    BARV(10);
    IW(10); IW(11); IW(12); IW(13); IW(14); IW(15);
    WFRAG(0); WFRAG(1); WFRAG(2);
    {
      const unsigned char* hp_ = ws + (u32)__builtin_amdgcn_readfirstlane((int)(WS_HA + (grp * 8 + wave) * 128 + 4 * (j ^ 2)));
      u32 n = 0;
      for (;;) {
        u32 hh;
        asm volatile("s_load_dword %0, %1, 0x0 glc\n\ts_waitcnt lgkmcnt(0)" : "=s"(hh) : "s"(hp_) : "memory");
        if (hh == tag32 || ++n > HINT_MAX) break;
        __builtin_amdgcn_s_sleep(1);
      }
    }
    const u32 s1 = (u32)((grp * 4 + (j ^ 2)) * 8 + wave);
    const unsigned char* ox = ws + WS_OX + (size_t)s1 * 4096u;
    const unsigned char* ol = ws + WS_OL + s1 * 384u;
    const u32 vo = lane * 16u, lo = rtl * 128u + l16 * 8u;
    u32x4 G[4];
    u32x2 Lg;
    u32 sp = 0;
#define GISSUE()                                                                                                   \
  do {                                                                                                             \
    GLD2(GPOL, Lg, lo, ol);                                                                                        \
    GLD(GPOL, G[0], vo, ox, 0); GLD(GPOL, G[1], vo, ox, 1024); GLD(GPOL, G[2], vo, ox, 2048); GLD(GPOL, G[3], vo, ox, 3072); \
  } while (0)
#define GWAIT() asm volatile("s_waitcnt vmcnt(0)" : "+v"(G[0]), "+v"(G[1]), "+v"(G[2]), "+v"(G[3]), "+v"(Lg)::"memory")
    auto gcheck = [&]() {
      const bool ok = (Lg[1] == tag32) & (G[0][3] == tag32) & (G[1][3] == tag32) & (G[2][3] == tag32) & (G[3][3] == tag32);
      return __all(ok) != 0;
    };
    GISSUE();
    BARV(5);
    WFRAG(3); WFRAG(4); WFRAG(5); WFRAG(6); WFRAG(7);
    GWAIT();
    if (!gcheck()) {
      for (;;) {
        GISSUE();
        GWAIT();
        if (gcheck() || ++sp > SPIN_MAX) break;
      }
    }
    const u32 bo = L0 + BFO + (u32)wave * 3072u + lane * 16u;
    float eav[3], ebv[3];
    {
      const float lb = __uint_as_float(Lg[0]);
      const float M = fmaxf(lse, lb);
      const float ea = ex2(lse - M), eb = ex2(lb - M);
      const float inv = __builtin_amdgcn_rcpf(ea + eb);
      bcast3(ea * inv, eav);
      bcast3(eb * inv, ebv);
    }
#pragma unroll
    for (int rt = 0; rt < 3; rt++) {
      const float ea = eav[rt], eb = ebv[rt];
      u32x4 v;
#pragma unroll
      for (int jj = 0; jj < 4; jj++)
        v[jj] = pk(Ff[rt][2 * jj] * ea + bflo(G[jj][rt]) * eb, Ff[rt][2 * jj + 1] * ea + bfhi(G[jj][rt]) * eb);
      st128(bo + rt * 1024, v);
    }
  }
  BAR();
  {
    const u32 bfr = L0 + BFO + lane * 16u;
    f32x4 acc[2][3];
#pragma unroll
    for (int i = 0; i < 8; i++) {
      u32x4 Bf[3];
#pragma unroll
      for (int rt = 0; rt < 3; rt++) Bf[rt] = lds128(bfr + (u32)(i * 3 + rt) * 1024u);
#pragma unroll
      for (int c = 0; c < 2; c++)
#pragma unroll
        for (int rt = 0; rt < 3; rt++) acc[c][rt] = mfma(wf[c][i], Bf[rt], i ? acc[c][rt] : f32x4{0, 0, 0, 0});
    }
    const int cb = b * 32 + 16 * kh + 2 * wave, src = 2 * kvh + hp;
    const u32 po = WS_OP + (u32)(cb * 32768 + src * 2048) + g4 * 512u + l16 * 16u;
#pragma unroll
    for (int c = 0; c < 2; c++)
#pragma unroll
      for (int e = 0; e < 2; e++) {
        u32x4 v = {pk(acc[c][0][2 * e], acc[c][0][2 * e + 1]), pk(acc[c][1][2 * e], acc[c][1][2 * e + 1]), pk(acc[c][2][2 * e], acc[c][2][2 * e + 1]), tag32};
        __builtin_amdgcn_raw_buffer_store_b128(v, rs, po + c * 32768u + e * 256u, 0, SAUX);
      }
    if (lane < 2) __hip_atomic_store((GAS u32*)(ws + WS_HB + cb * 128 + src * 4 + lane * 128u), tag32, __ATOMIC_RELAXED, __HIP_MEMORY_SCOPE_AGENT);
  }
  RELANE();
  {
    const int cb = b * 32 + 8 * j + kvh;
    {
      const unsigned char* hp_ = ws + WS_HB + cb * 128;
      u32 n = 0;
      for (;;) {
        typedef u32 u32x16 __attribute__((ext_vector_type(16)));
        u32x16 hh;
        asm volatile("s_load_dwordx16 %0, %1, 0x0 glc\n\ts_waitcnt lgkmcnt(0)" : "=s"(hh) : "s"(hp_) : "memory");
        u32 df = 0;
#pragma unroll
        for (int i = 0; i < 16; i++) df |= hh[i] ^ tag32;
        if (df == 0 || ++n > HINT_MAX) break;
        __builtin_amdgcn_s_sleep(1);
      }
    }
    const int e = wave & 1, gq = wave >> 1;
    const unsigned char* pb = ws + WS_OP + (size_t)(cb * 32768 + gq * 512 + e * 256);
    const u32 vo = g4 * 2048u + l16 * 16u;
    u32x4 G[4];
    u32 sp = 0;
    for (;;) {
      GLD(GPOL, G[0], vo, pb, 0);
      GLD(GPOL, G[1], vo, pb + 8192, 0);
      GLD(GPOL, G[2], vo, pb + 16384, 0);
      GLD(GPOL, G[3], vo, pb + 24576, 0);
      asm volatile("s_waitcnt vmcnt(0)" : "+v"(G[0]), "+v"(G[1]), "+v"(G[2]), "+v"(G[3])::"memory");
      if (__all((G[0][3] == tag32) & (G[1][3] == tag32) & (G[2][3] == tag32) & (G[3][3] == tag32))) break;
      if (++sp > SPIN_MAX) break;
    }
    if (bid == b * 32 && wave == 0 && lane == 0) __hip_atomic_store((GAS u64*)(ws + WS_G + b * 128), tag, __ATOMIC_RELAXED, __HIP_MEMORY_SCOPE_AGENT);
    u32 res = 0;
#pragma unroll
    for (int rt = 0; rt < 3; rt++) {
      const float lo_ = xsum(addh(G[3][rt], slo, addh(G[2][rt], slo, addh(G[1][rt], slo, bflo(G[0][rt])))));
      const float hi_ = xsum(addh(G[3][rt], shi, addh(G[2][rt], shi, addh(G[1][rt], shi, bfhi(G[0][rt])))));
      const u32 p = pk(lo_, hi_);
      if (g4 == (u32)rt) res = p;
    }
    const u32 row = 16u * g4 + l16, col = 128u * j + 16u * kvh + 4u * gq + 2u * e;
    if (g4 < 3) *(GAS u32*)(Out + (size_t)(b * 48 + row) * 512 + col) = res;
  }
}

extern "C" void kernel_launch(void* const* d_in, const int* in_sizes, int n_in, void* d_out, int out_size, void* d_ws, size_t ws_size,
                              hipStream_t stream) {
  (void)in_sizes; (void)n_in; (void)out_size; (void)ws_size;
  (void)hipFuncSetAttribute((const void*)gqa_fused, hipFuncAttributeMaxDynamicSharedMemorySize, 163840);
  gqa_fused<<<256, 512, 163840, stream>>>((const u16*)d_in[0], (const u16*)d_in[1], (const u16*)d_in[2], (const u16*)d_in[3],
                                          (const u16*)d_in[4], (u16*)d_out, (unsigned char*)d_ws);
}
```
